```python
import torch

def _softmax(x, axis=-1):
    return torch.softmax(x, dim=axis)

def _gelu(x):
    return torch.nn.functional.gelu(x, approximate='tanh')

def _silu(x):
    return torch.nn.functional.silu(x)

def _sigmoid(x):
    return torch.sigmoid(x)

def _layernorm(x, axis=-1, eps=1e-05):
    m = x.mean(dim=axis, keepdim=True)
    v = x.var(dim=axis, keepdim=True, unbiased=False)
    return (x - m) / torch.sqrt(v + eps)

def _scatter_add(tgt, idx, src, axis=0):
    return tgt.clone().index_add_(axis, idx, src)

def _segment_sum(src, seg, n):
    out = torch.zeros((n,) + src.shape[1:], dtype=src.dtype)
    return out.index_add_(0, seg, src)

def _pad(x, pw):
    flat = tuple((v for pair in reversed(pw) for v in pair))
    return torch.nn.functional.pad(x, flat)
kernel_name = 'concat_heads__ssm_conv_s5__memory_aug_recurrent_mem_tr__prompt'

def setup_inputs():

    _w = lambda a: (lambda t: t.to(torch.bfloat16) if t.is_floating_point() else t)(torch.as_tensor(a))

    def _A_setup():
        B, S, D = (1, 2048, 512)
        P = D
        Lambda_re = -0.5 * (lambda _x: torch.exp(torch.as_tensor(_x)))((lambda *s: torch.randn(*s, dtype=torch.bfloat16) * 0.8)(P))
        Lambda_im = (lambda *s: torch.randn(*s, dtype=torch.bfloat16) * 0.8)(P) * 1.0
        B_re = (lambda *s: torch.randn(*s, dtype=torch.bfloat16) * 0.8)(P, D) * D ** (-0.5)
        B_im = (lambda *s: torch.randn(*s, dtype=torch.bfloat16) * 0.8)(P, D) * D ** (-0.5)
        C_re = (lambda *s: torch.randn(*s, dtype=torch.bfloat16) * 0.8)(D, P) * P ** (-0.5)
        C_im = (lambda *s: torch.randn(*s, dtype=torch.bfloat16) * 0.8)(D, P) * P ** (-0.5)
        log_dt = (lambda *s: torch.randn(*s, dtype=torch.bfloat16) * 0.8)(P) * 0.3 - 2.0
        Dskip = (lambda *s: torch.randn(*s, dtype=torch.bfloat16) * 0.8)(D)
        return {'x': _w((lambda *s: torch.randn(*s, dtype=torch.bfloat16) * 0.05)(B, S, D)), 'Lambda_re': _w(Lambda_re), 'Lambda_im': _w(Lambda_im), 'B_re': _w(B_re), 'B_im': _w(B_im), 'C_re': _w(C_re), 'C_im': _w(C_im), 'log_dt': _w(log_dt), 'Dskip': _w(Dskip)}
    _A_in = _A_setup()

    def _B_setup():
        B, S, Dm = (1, 2048, 512)
        H, Dh = (32, 128)
        M, seg = (8, 32)
        seg = min(seg, S)
        S = S // seg * seg
        return {'x': _w((lambda *s: torch.randn(*s, dtype=torch.bfloat16) * 0.05)(B, S, Dm)), 'mem0': _w((lambda *s: torch.randn(*s, dtype=torch.bfloat16) * 0.8)(B, M, Dm)), 'Wq': _w((lambda *s: torch.randn(*s, dtype=torch.bfloat16) * 1.6)(Dm, H, Dh) * Dm ** (-0.5)), 'Wk': _w((lambda *s: torch.randn(*s, dtype=torch.bfloat16) * 1.6)(Dm, H, Dh) * Dm ** (-0.5)), 'Wv': _w((lambda *s: torch.randn(*s, dtype=torch.bfloat16) * 0.8)(Dm, H, Dh) * Dm ** (-0.5)), 'Wo': _w((lambda *s: torch.randn(*s, dtype=torch.bfloat16) * 0.8)(H, Dh, Dm) * (H * Dh) ** (-0.5)), 'seg_len': seg}
    _B_in = _B_setup()
    return {'A_x': _A_in['x'], 'A_Lambda_re': _A_in['Lambda_re'], 'A_Lambda_im': _A_in['Lambda_im'], 'A_B_re': _A_in['B_re'], 'A_B_im': _A_in['B_im'], 'A_C_re': _A_in['C_re'], 'A_C_im': _A_in['C_im'], 'A_log_dt': _A_in['log_dt'], 'A_Dskip': _A_in['Dskip'], 'B_x': _B_in['x'], 'B_mem0': _B_in['mem0'], 'B_Wq': _B_in['Wq'], 'B_Wk': _B_in['Wk'], 'B_Wv': _B_in['Wv'], 'B_Wo': _B_in['Wo'], 'B_seg_len': _B_in['seg_len']}

def reference(A_x, A_Lambda_re, A_Lambda_im, A_B_re, A_B_im, A_C_re, A_C_im, A_log_dt, A_Dskip, B_x, B_mem0, B_Wq, B_Wk, B_Wv, B_Wo, B_seg_len):

    def _A_ref(x, Lambda_re, Lambda_im, B_re, B_im, C_re, C_im, log_dt, Dskip):
        Bsz, L, H = x.shape
        Lam = Lambda_re + 1j * Lambda_im
        Bmat = B_re + 1j * B_im
        Cmat = C_re + 1j * C_im
        dt = torch.exp(log_dt)
        Lam_bar = torch.exp(dt * Lam)
        B_bar = ((Lam_bar - 1.0) / Lam)[:, None] * Bmat
        Bu = torch.einsum('bld,pd->blp', x + 0j, B_bar)
        h = torch.zeros_like(Bu[:, 0, :])
        hs = []
        for t in range(L):
            h = Lam_bar[None, :] * h + Bu[:, t, :]
            hs.append(h)
        hseq = torch.stack(hs, 1)
        y = 2.0 * torch.einsum('blp,dp->bld', hseq, Cmat).real + x * Dskip[None, None, :]
        return y

    def _B_ref(x, mem0, Wq, Wk, Wv, Wo, seg_len):
        B, S, Dm = x.shape
        H, Dh = (Wq.shape[1], Wq.shape[2])
        M = mem0.shape[1]
        scale = Dh ** (-0.5)
        n_seg = S // seg_len
        L = M + seg_len
        idx = torch.arange(L)
        mask = (idx[:, None] < M) | (idx[:, None] >= idx[None, :])
        mem = mem0
        outs = []
        for s in range(n_seg):
            xs = x[:, s * seg_len:(s + 1) * seg_len, :].clone()
            h = torch.concatenate([mem, xs], axis=1)
            q = torch.einsum('btd,dhe->bhte', h, Wq)
            k = torch.einsum('btd,dhe->bhte', h, Wk)
            v = torch.einsum('btd,dhe->bhte', h, Wv)
            scores = torch.einsum('bhqe,bhke->bhqk', q, k) * scale
            scores = torch.where(mask[None, None], scores, -1000000000.0)
            p = _softmax(scores, axis=-1)
            o = torch.einsum('bhqk,bhke->bhqe', p, v)
            o = torch.einsum('bhte,hed->btd', o, Wo)
            mem = o[:, :M, :].clone()
            outs.append(o[:, M:, :])
        return torch.concatenate(outs, axis=1)
    a = _A_ref(A_x, A_Lambda_re, A_Lambda_im, A_B_re, A_B_im, A_C_re, A_C_im, A_log_dt, A_Dskip)
    b = _B_ref(B_x, B_mem0, B_Wq, B_Wk, B_Wv, B_Wo, B_seg_len)
    return torch.concatenate([a, b], axis=-1)
```

```cpp
#include <hip/hip_runtime.h>
#include <hip/hip_bf16.h>
typedef unsigned short u16;
__device__ __forceinline__ float b2f(u16 v) { return __uint_as_float(((unsigned)v) << 16); }
__device__ __forceinline__ u16 f2b(float f) { unsigned u = __float_as_uint(f); return (u16)((u + 0x7fffu + ((u >> 16) & 1u)) >> 16); }
__device__ __forceinline__ float rb(float f) { return b2f(f2b(f)); }
constexpr int S = 2048, D = 512, P = 512, H = 32, DH = 128, M = 8, SEG = 32, NS = S / SEG, HD = H * DH;

__global__ void k_bu(const u16* x, const u16* lre, const u16* lim, const u16* bre, const u16* bim, const u16* ldt, float* bur, float* bui) {
    int p = blockIdx.x * blockDim.x + threadIdx.x, l = blockIdx.y;
    float sr = 0, si = 0;
    for (int d = 0; d < D; ++d) { float xv = b2f(x[l * D + d]); sr += xv * b2f(bre[p * D + d]); si += xv * b2f(bim[p * D + d]); }
    float dt = rb(expf(b2f(ldt[p]))), a = b2f(lre[p]), b = b2f(lim[p]);
    float e = expf(dt * a), lr = e * cosf(dt * b), li = e * sinf(dt * b);
    float nr = lr - 1.f, ni = li, den = a * a + b * b;
    float cr = (nr * a + ni * b) / den, ci = (ni * a - nr * b) / den;
    bur[l * P + p] = cr * sr - ci * si; bui[l * P + p] = cr * si + ci * sr;
}
__global__ void k_scan(const u16* lre, const u16* lim, const u16* ldt, float* bur, float* bui) {
    int p = blockIdx.x * blockDim.x + threadIdx.x;
    float dt = rb(expf(b2f(ldt[p]))), a = b2f(lre[p]), b = b2f(lim[p]);
    float e = expf(dt * a), lr = e * cosf(dt * b), li = e * sinf(dt * b);
    float hr = 0, hi = 0;
    for (int l = 0; l < S; ++l) { float nr = lr * hr - li * hi + bur[l * P + p], ni = lr * hi + li * hr + bui[l * P + p]; hr = nr; hi = ni; bur[l * P + p] = hr; bui[l * P + p] = hi; }
}
__global__ void k_y(const u16* x, const u16* cre, const u16* cim, const u16* dsk, const float* hr, const float* hi, float* out) {
    int d = blockIdx.x * blockDim.x + threadIdx.x, l = blockIdx.y;
    float s = 0;
    for (int p = 0; p < P; ++p) s += hr[l * P + p] * b2f(cre[d * P + p]) - hi[l * P + p] * b2f(cim[d * P + p]);
    out[l * 1024 + d] = 2.f * s + rb(b2f(x[l * D + d]) * b2f(dsk[d]));
}
__global__ void k_proj(const u16* x, const u16* wq, const u16* wk, const u16* wv, u16* q, u16* k, u16* v) {
    int c = blockIdx.x * blockDim.x + threadIdx.x, l = blockIdx.y;
    float sq = 0, sk = 0, sv = 0;
    for (int d = 0; d < D; ++d) { float xv = b2f(x[l * D + d]); sq += xv * b2f(wq[d * HD + c]); sk += xv * b2f(wk[d * HD + c]); sv += xv * b2f(wv[d * HD + c]); }
    q[l * HD + c] = f2b(sq); k[l * HD + c] = f2b(sk); v[l * HD + c] = f2b(sv);
}
__global__ void k_attn(const u16* qm, const u16* km, const u16* vm, const u16* qx, const u16* kx, const u16* vx, u16* o) {
    int h = blockIdx.x, t = blockIdx.y, e = threadIdx.x;
    __shared__ float sc[40];
    const u16* q = (t < M ? qm + t * HD : qx + (t - M) * HD) + h * DH;
    if (e < 40) {
        const u16* k = (e < M ? km + e * HD : kx + (e - M) * HD) + h * DH;
        float s = 0; for (int i = 0; i < DH; ++i) s += b2f(q[i]) * b2f(k[i]);
        s = rb(rb(s) * rb(0.08838834764831845f));
        if (!(t < M || t >= e)) s = -1e9f;
        sc[e] = s;
    }
    __syncthreads();
    float mx = -3e38f; for (int j = 0; j < 40; ++j) mx = fmaxf(mx, sc[j]);
    float sum = 0; for (int j = 0; j < 40; ++j) sum += expf(sc[j] - mx);
    float acc = 0;
    for (int j = 0; j < 40; ++j) { float pj = rb(expf(sc[j] - mx) / sum); const u16* v = (j < M ? vm + j * HD : vx + (j - M) * HD) + h * DH; acc += pj * b2f(v[e]); }
    o[t * HD + h * DH + e] = f2b(acc);
}
__global__ void k_oproj(const u16* o, const u16* wo, u16* mem, float* out) {
    int d = blockIdx.x * blockDim.x + threadIdx.x, t = blockIdx.y;
    float s = 0; for (int c = 0; c < HD; ++c) s += b2f(o[t * HD + c]) * b2f(wo[c * D + d]);
    if (t < M) mem[t * D + d] = f2b(s); else out[(t - M) * 1024 + 512 + d] = rb(s);
}
extern "C" void kernel_launch(void* const* d_in, const int* in_sizes, int n_in, void* d_out, int out_size, void* d_ws, size_t ws_size, hipStream_t stream) {
    const u16* const* in = (const u16* const*)d_in;
    float* out = (float*)d_out; char* ws = (char*)d_ws;
    float* bur = (float*)ws; float* bui = bur + S * P;
    u16* q = (u16*)(bui + S * P); u16* k = q + S * HD; u16* v = k + S * HD;
    u16* qm = v + S * HD; u16* km = qm + M * HD; u16* vm = km + M * HD; u16* o = vm + M * HD; u16* mem = o + 40 * HD;
    k_bu<<<dim3(P / 256, S), 256, 0, stream>>>(in[0], in[1], in[2], in[3], in[4], in[7], bur, bui);
    k_scan<<<P / 64, 64, 0, stream>>>(in[1], in[2], in[7], bur, bui);
    k_y<<<dim3(D / 256, S), 256, 0, stream>>>(in[0], in[5], in[6], in[8], bur, bui, out);
    k_proj<<<dim3(HD / 256, S), 256, 0, stream>>>(in[9], in[11], in[12], in[13], q, k, v);
    for (int s = 0; s < NS; ++s) {
        k_proj<<<dim3(HD / 256, M), 256, 0, stream>>>(s == 0 ? in[10] : mem, in[11], in[12], in[13], qm, km, vm);
        k_attn<<<dim3(H, 40), 128, 0, stream>>>(qm, km, vm, q + s * SEG * HD, k + s * SEG * HD, v + s * SEG * HD, o);
        k_oproj<<<dim3(D / 64, 40), 64, 0, stream>>>(o, in[14], mem, out + s * SEG * 1024);
    }
}
```

```cpp
#include <hip/hip_runtime.h>
#pragma clang diagnostic ignored "-Winline-asm"
#pragma clang diagnostic ignored "-Wint-to-pointer-cast"

#define CHAIN_ASM \
"s_mov_b32 s76, 0\n" \
"s_mov_b32 s46, 0x00ff00ff\n" \
"s_mov_b32 s47, 0x00ff00ff\n" \
"s_load_dwordx16 s[80:95], %[ka], 72\n" \
"s_waitcnt lgkmcnt(0)\n" \
"s_mov_b32 s52, s94\n" \
"s_and_b32 s53, s95, 0xffff\n" \
"s_mov_b32 s54, 67108864\n" \
"s_mov_b32 s55, 0x20000\n" \
"s_bfe_u32 s96, %[id], 524304\n" \
"s_lshl_b32 s96, s96, 5\n" \
"s_bfe_u32 s99, %[id], 131072\n" \
"s_add_u32 s96, s96, s99\n" \
"s_lshl_b32 s96, s96, 2\n" \
"s_add_u32 s99, s96, 2719744\n" \
"buffer_load_dword v125, off, s[52:55], s99 sc1\n" \
"s_bfe_u32 s36, %[id], 131072\n" \
"s_bfe_u32 s37, %[id], 327682\n" \
"s_bfe_u32 s38, %[id], 65543\n" \
"s_mov_b32 s43, %[da]\n" \
"s_mov_b32 s44, %[db]\n" \
"s_mov_b32 s45, %[tq]\n" \
"s_mov_b32 s77, 376\n" \
"v_mbcnt_lo_u32_b32 v126, -1, 0\n" \
"v_mbcnt_hi_u32_b32 v126, -1, v126\n" \
"v_bfe_u32 v127, v126, 2, 1\n" \
"v_mul_u32_u24 v112, 131072, v127\n" \
"v_bfe_u32 v127, v126, 5, 1\n" \
"v_mul_u32_u24 v127, 32768, v127\n" \
"v_add_u32 v112, v112, v127\n" \
"v_bfe_u32 v127, v126, 3, 1\n" \
"v_mul_u32_u24 v127, 16384, v127\n" \
"v_add_u32 v112, v112, v127\n" \
"v_bfe_u32 v127, v126, 4, 1\n" \
"v_mul_u32_u24 v127, 8224, v127\n" \
"v_add_u32 v112, v112, v127\n" \
"v_bfe_u32 v127, v126, 0, 1\n" \
"v_mul_u32_u24 v127, 16, v127\n" \
"v_add_u32 v112, v112, v127\n" \
"v_and_b32 v127, 2, v126\n" \
"v_lshlrev_b32 v127, 4, v127\n" \
"v_xor_b32 v112, v112, v127\n" \
"v_add_u32 v115, 324608, v112\n" \
"v_add_u32 v114, 63488, v112\n" \
"v_add_u32 v113, 261120, v112\n" \
"v_bfe_u32 v127, v126, 5, 1\n" \
"v_mul_u32_u24 v116, 32768, v127\n" \
"v_bfe_u32 v127, v126, 4, 1\n" \
"v_mul_u32_u24 v127, 16384, v127\n" \
"v_add_u32 v116, v116, v127\n" \
"v_bfe_u32 v127, v126, 3, 1\n" \
"v_mul_u32_u24 v127, 1024, v127\n" \
"v_add_u32 v116, v116, v127\n" \
"v_bfe_u32 v127, v126, 4, 2\n" \
"v_mul_u32_u24 v127, 32, v127\n" \
"v_add_u32 v116, v116, v127\n" \
"v_bfe_u32 v127, v126, 0, 1\n" \
"v_mul_u32_u24 v127, 16, v127\n" \
"v_add_u32 v116, v116, v127\n" \
"v_and_b32 v127, 6, v126\n" \
"v_lshlrev_b32 v127, 4, v127\n" \
"v_xor_b32 v116, v116, v127\n" \
"v_add_u32 v119, 3072, v116\n" \
"v_add_u32 v118, 0, v116\n" \
"v_add_u32 v117, 3072, v116\n" \
"s_mul_i32 s62, s36, 40960\n" \
"v_bfe_u32 v127, v126, 4, 2\n" \
"v_mul_u32_u24 v120, 256, v127\n" \
"v_bfe_u32 v127, v126, 2, 2\n" \
"v_mul_u32_u24 v127, 64, v127\n" \
"v_add_u32 v120, v120, v127\n" \
"v_bfe_u32 v127, v126, 4, 1\n" \
"v_mul_u32_u24 v127, 32, v127\n" \
"v_add_u32 v120, v120, v127\n" \
"v_bfe_u32 v127, v126, 0, 2\n" \
"v_mul_u32_u24 v127, 8, v127\n" \
"v_add_u32 v120, v120, v127\n" \
"v_add_u32 v120, s62, v120\n" \
"v_xor_b32 v121, 32, v120\n" \
"v_bfe_u32 v127, v126, 5, 1\n" \
"v_mul_u32_u24 v106, 1024, v127\n" \
"v_bfe_u32 v127, v126, 4, 1\n" \
"v_mul_u32_u24 v127, 512, v127\n" \
"v_add_u32 v106, v106, v127\n" \
"v_bfe_u32 v127, v126, 2, 2\n" \
"v_mul_u32_u24 v127, 128, v127\n" \
"v_add_u32 v106, v106, v127\n" \
"v_bfe_u32 v127, v126, 3, 2\n" \
"v_mul_u32_u24 v127, 32, v127\n" \
"v_add_u32 v106, v106, v127\n" \
"v_bfe_u32 v127, v126, 0, 2\n" \
"v_mul_u32_u24 v127, 8, v127\n" \
"v_add_u32 v106, v106, v127\n" \
"v_add_u32 v106, s62, v106\n" \
"v_xor_b32 v107, 32, v106\n" \
"v_xor_b32 v108, 64, v106\n" \
"v_xor_b32 v109, 96, v106\n" \
"s_lshl_b32 s75, s36, 1\n" \
"s_lshl_b32 s68, s37, 8\n" \
"s_lshl_b32 s69, s36, 6\n" \
"s_add_u32 s71, s68, s69\n" \
"s_lshl_b32 s68, s37, 17\n" \
"s_lshl_b32 s69, s38, 9\n" \
"s_add_u32 s68, s68, s69\n" \
"s_lshl_b32 s69, s36, 7\n" \
"s_add_u32 s61, s68, s69\n" \
"s_mov_b32 s48, s86\n" \
"s_and_b32 s49, s87, 0xffff\n" \
"s_mov_b32 s50, 4194304\n" \
"s_mov_b32 s51, 0x20000\n" \
"s_xor_b32 s74, s75, 0\n" \
"s_lshl_b32 s74, s74, 19\n" \
"s_add_u32 s74, s74, s71\n" \
"s_add_u32 m0, s62, 0\n" \
"s_nop 0\n" \
"buffer_load_dwordx4 v112, s[48:51], s74 offen offset:0 lds\n" \
"buffer_load_dwordx4 v113, s[48:51], s74 offen offset:1024 lds\n" \
"buffer_load_dwordx4 v114, s[48:51], s74 offen offset:2048 lds\n" \
"buffer_load_dwordx4 v115, s[48:51], s74 offen offset:3072 lds\n" \
"s_xor_b32 s74, s75, 1\n" \
"s_lshl_b32 s74, s74, 19\n" \
"s_add_u32 s74, s74, s71\n" \
"s_add_u32 m0, s62, 4096\n" \
"s_nop 0\n" \
"buffer_load_dwordx4 v112, s[48:51], s74 offen offset:0 lds\n" \
"buffer_load_dwordx4 v113, s[48:51], s74 offen offset:1024 lds\n" \
"buffer_load_dwordx4 v114, s[48:51], s74 offen offset:2048 lds\n" \
"buffer_load_dwordx4 v115, s[48:51], s74 offen offset:3072 lds\n" \
"s_xor_b32 s74, s75, 2\n" \
"s_lshl_b32 s74, s74, 19\n" \
"s_add_u32 s74, s74, s71\n" \
"s_add_u32 m0, s62, 8192\n" \
"s_nop 0\n" \
"buffer_load_dwordx4 v112, s[48:51], s74 offen offset:0 lds\n" \
"buffer_load_dwordx4 v113, s[48:51], s74 offen offset:1024 lds\n" \
"buffer_load_dwordx4 v114, s[48:51], s74 offen offset:2048 lds\n" \
"buffer_load_dwordx4 v115, s[48:51], s74 offen offset:3072 lds\n" \
"s_xor_b32 s74, s75, 3\n" \
"s_lshl_b32 s74, s74, 19\n" \
"s_add_u32 s74, s74, s71\n" \
"s_add_u32 m0, s62, 12288\n" \
"s_nop 0\n" \
"buffer_load_dwordx4 v112, s[48:51], s74 offen offset:0 lds\n" \
"buffer_load_dwordx4 v113, s[48:51], s74 offen offset:1024 lds\n" \
"buffer_load_dwordx4 v114, s[48:51], s74 offen offset:2048 lds\n" \
"buffer_load_dwordx4 v115, s[48:51], s74 offen offset:3072 lds\n" \
"s_xor_b32 s74, s75, 4\n" \
"s_lshl_b32 s74, s74, 19\n" \
"s_add_u32 s74, s74, s71\n" \
"s_add_u32 m0, s62, 16384\n" \
"s_nop 0\n" \
"buffer_load_dwordx4 v112, s[48:51], s74 offen offset:0 lds\n" \
"buffer_load_dwordx4 v113, s[48:51], s74 offen offset:1024 lds\n" \
"buffer_load_dwordx4 v114, s[48:51], s74 offen offset:2048 lds\n" \
"buffer_load_dwordx4 v115, s[48:51], s74 offen offset:3072 lds\n" \
"s_xor_b32 s74, s75, 5\n" \
"s_lshl_b32 s74, s74, 19\n" \
"s_add_u32 s74, s74, s71\n" \
"s_add_u32 m0, s62, 20480\n" \
"s_nop 0\n" \
"buffer_load_dwordx4 v112, s[48:51], s74 offen offset:0 lds\n" \
"buffer_load_dwordx4 v113, s[48:51], s74 offen offset:1024 lds\n" \
"buffer_load_dwordx4 v114, s[48:51], s74 offen offset:2048 lds\n" \
"buffer_load_dwordx4 v115, s[48:51], s74 offen offset:3072 lds\n" \
"s_xor_b32 s74, s75, 6\n" \
"s_lshl_b32 s74, s74, 19\n" \
"s_add_u32 s74, s74, s71\n" \
"s_add_u32 m0, s62, 24576\n" \
"s_nop 0\n" \
"buffer_load_dwordx4 v112, s[48:51], s74 offen offset:0 lds\n" \
"buffer_load_dwordx4 v113, s[48:51], s74 offen offset:1024 lds\n" \
"buffer_load_dwordx4 v114, s[48:51], s74 offen offset:2048 lds\n" \
"buffer_load_dwordx4 v115, s[48:51], s74 offen offset:3072 lds\n" \
"s_xor_b32 s74, s75, 7\n" \
"s_lshl_b32 s74, s74, 19\n" \
"s_add_u32 s74, s74, s71\n" \
"s_add_u32 m0, s62, 28672\n" \
"s_nop 0\n" \
"buffer_load_dwordx4 v112, s[48:51], s74 offen offset:0 lds\n" \
"buffer_load_dwordx4 v113, s[48:51], s74 offen offset:1024 lds\n" \
"buffer_load_dwordx4 v114, s[48:51], s74 offen offset:2048 lds\n" \
"buffer_load_dwordx4 v115, s[48:51], s74 offen offset:3072 lds\n" \
"s_mov_b32 s48, s88\n" \
"s_and_b32 s49, s89, 0xffff\n" \
"s_mov_b32 s50, 4194304\n" \
"s_mov_b32 s51, 0x20000\n" \
"s_xor_b32 s74, s75, 0\n" \
"s_lshl_b32 s74, s74, 19\n" \
"s_add_u32 s74, s74, s71\n" \
"s_add_u32 m0, s62, 32768\n" \
"s_nop 0\n" \
"buffer_load_dwordx4 v112, s[48:51], s74 offen offset:0 lds\n" \
"buffer_load_dwordx4 v113, s[48:51], s74 offen offset:1024 lds\n" \
"buffer_load_dwordx4 v114, s[48:51], s74 offen offset:2048 lds\n" \
"buffer_load_dwordx4 v115, s[48:51], s74 offen offset:3072 lds\n" \
"s_xor_b32 s74, s75, 1\n" \
"s_lshl_b32 s74, s74, 19\n" \
"s_add_u32 s74, s74, s71\n" \
"s_add_u32 m0, s62, 36864\n" \
"s_nop 0\n" \
"buffer_load_dwordx4 v112, s[48:51], s74 offen offset:0 lds\n" \
"buffer_load_dwordx4 v113, s[48:51], s74 offen offset:1024 lds\n" \
"buffer_load_dwordx4 v114, s[48:51], s74 offen offset:2048 lds\n" \
"buffer_load_dwordx4 v115, s[48:51], s74 offen offset:3072 lds\n" \
"v_bfe_u32 v127, v126, 1, 1\n" \
"v_mul_u32_u24 v0, 0xaaaa, v127\n" \
"v_add_u32 v0, 0x4444, v0\n" \
"v_lshlrev_b32 v1, 4, v126\n" \
"s_lshl_b32 s68, s36, 11\n" \
"v_add_u32 v3, s68, v1\n" \
"s_lshl_b32 s68, s36, 8\n" \
"s_add_u32 s68, s68, 40960\n" \
"v_bfe_u32 v127, v126, 4, 2\n" \
"v_mul_u32_u24 v4, 2112, v127\n" \
"v_bfe_u32 v127, v126, 0, 4\n" \
"v_mul_u32_u24 v127, 8, v127\n" \
"v_add_u32 v4, v4, v127\n" \
"v_add_u32 v4, s68, v4\n" \
"v_bfe_u32 v127, v126, 0, 2\n" \
"v_mul_u32_u24 v5, 2112, v127\n" \
"v_bfe_u32 v127, v126, 2, 4\n" \
"v_mul_u32_u24 v127, 8, v127\n" \
"v_add_u32 v5, v5, v127\n" \
"v_add_u32 v5, 40960, v5\n" \
"s_lshl_b32 s68, s36, 14\n" \
"s_add_u32 s68, s68, 98304\n" \
"v_add_u32 v6, s68, v1\n" \
"v_mov_b32 v8, 0x3f803f80\n" \
"v_mov_b32 v12, 0\n" \
"v_mov_b32 v9, 0x3f803f80\n" \
"v_mov_b32 v13, 0\n" \
"v_mov_b32 v10, 0x3f803f80\n" \
"v_mov_b32 v14, 0\n" \
"v_mov_b32 v11, 0x3f803f80\n" \
"v_mov_b32 v15, 0\n" \
"v_and_b32 v122, 3, v126\n" \
"v_bfe_u32 v123, v126, 2, 1\n" \
"v_mul_u32_u24 v123, 16, v123\n" \
"v_mov_b32 v124, 0x3f80\n" \
"v_lshlrev_b32 v123, v123, v124\n" \
"v_cmp_eq_u32 vcc, 0, v122\n" \
"s_nop 1\n" \
"v_cndmask_b32 v100, 0, v123, vcc\n" \
"v_cmp_eq_u32 vcc, 1, v122\n" \
"s_nop 1\n" \
"v_cndmask_b32 v101, 0, v123, vcc\n" \
"v_cmp_eq_u32 vcc, 2, v122\n" \
"s_nop 1\n" \
"v_cndmask_b32 v102, 0, v123, vcc\n" \
"v_cmp_eq_u32 vcc, 3, v122\n" \
"s_nop 1\n" \
"v_cndmask_b32 v103, 0, v123, vcc\n" \
"v_bfe_u32 v127, v126, 2, 2\n" \
"v_mul_u32_u24 v105, 2048, v127\n" \
"v_bfe_u32 v127, v126, 0, 1\n" \
"v_mul_u32_u24 v127, 1024, v127\n" \
"v_add_u32 v105, v105, v127\n" \
"v_bfe_u32 v127, v126, 4, 2\n" \
"v_mul_u32_u24 v127, 8, v127\n" \
"v_add_u32 v105, v105, v127\n" \
"v_bfe_u32 v127, v126, 1, 1\n" \
"v_mul_u32_u24 v127, 4, v127\n" \
"v_add_u32 v105, v105, v127\n" \
"s_waitcnt vmcnt(40)\n" \
"v_readfirstlane_b32 s97, v125\n" \
"s_add_u32 s97, s97, 256\n" \
"s_lshr_b32 s97, s97, 8\n" \
"v_mov_b32 v125, s97\n" \
"s_add_u32 s99, s96, 2686976\n" \
"s_mov_b64 exec, 1\n" \
"buffer_store_dword v125, off, s[52:55], s99 sc1\n" \
"s_mov_b64 exec, -1\n" \
"s_waitcnt vmcnt(37)\n" \
"ds_read_b64_tr_b16 a[128:129], v120 offset:0\n" \
"ds_read_b64_tr_b16 a[130:131], v120 offset:1024\n" \
"ds_read_b64_tr_b16 a[132:133], v120 offset:2048\n" \
"ds_read_b64_tr_b16 a[134:135], v120 offset:3072\n" \
"ds_read_b64_tr_b16 a[192:193], v121 offset:0\n" \
"ds_read_b64_tr_b16 a[194:195], v121 offset:1024\n" \
"ds_read_b64_tr_b16 a[196:197], v121 offset:2048\n" \
"ds_read_b64_tr_b16 a[198:199], v121 offset:3072\n" \
"s_waitcnt vmcnt(33)\n" \
"ds_read_b64_tr_b16 a[136:137], v120 offset:4096\n" \
"ds_read_b64_tr_b16 a[138:139], v120 offset:5120\n" \
"ds_read_b64_tr_b16 a[140:141], v120 offset:6144\n" \
"ds_read_b64_tr_b16 a[142:143], v120 offset:7168\n" \
"s_waitcnt lgkmcnt(4)\n" \
"ds_read_b64_tr_b16 a[200:201], v121 offset:4096\n" \
"ds_read_b64_tr_b16 a[202:203], v121 offset:5120\n" \
"ds_read_b64_tr_b16 a[204:205], v121 offset:6144\n" \
"ds_read_b64_tr_b16 a[206:207], v121 offset:7168\n" \
"s_xor_b32 s74, s75, 2\n" \
"s_lshl_b32 s74, s74, 19\n" \
"s_add_u32 s74, s74, s71\n" \
"s_add_u32 m0, s62, 0\n" \
"s_nop 0\n" \
"buffer_load_dwordx4 v112, s[48:51], s74 offen offset:0 lds\n" \
"buffer_load_dwordx4 v113, s[48:51], s74 offen offset:1024 lds\n" \
"buffer_load_dwordx4 v114, s[48:51], s74 offen offset:2048 lds\n" \
"buffer_load_dwordx4 v115, s[48:51], s74 offen offset:3072 lds\n" \
"s_waitcnt vmcnt(33)\n" \
"ds_read_b64_tr_b16 a[144:145], v120 offset:8192\n" \
"ds_read_b64_tr_b16 a[146:147], v120 offset:9216\n" \
"ds_read_b64_tr_b16 a[148:149], v120 offset:10240\n" \
"ds_read_b64_tr_b16 a[150:151], v120 offset:11264\n" \
"s_waitcnt lgkmcnt(4)\n" \
"ds_read_b64_tr_b16 a[208:209], v121 offset:8192\n" \
"ds_read_b64_tr_b16 a[210:211], v121 offset:9216\n" \
"ds_read_b64_tr_b16 a[212:213], v121 offset:10240\n" \
"ds_read_b64_tr_b16 a[214:215], v121 offset:11264\n" \
"s_xor_b32 s74, s75, 3\n" \
"s_lshl_b32 s74, s74, 19\n" \
"s_add_u32 s74, s74, s71\n" \
"s_add_u32 m0, s62, 4096\n" \
"s_nop 0\n" \
"buffer_load_dwordx4 v112, s[48:51], s74 offen offset:0 lds\n" \
"buffer_load_dwordx4 v113, s[48:51], s74 offen offset:1024 lds\n" \
"buffer_load_dwordx4 v114, s[48:51], s74 offen offset:2048 lds\n" \
"buffer_load_dwordx4 v115, s[48:51], s74 offen offset:3072 lds\n" \
"s_waitcnt vmcnt(33)\n" \
"ds_read_b64_tr_b16 a[152:153], v120 offset:12288\n" \
"ds_read_b64_tr_b16 a[154:155], v120 offset:13312\n" \
"ds_read_b64_tr_b16 a[156:157], v120 offset:14336\n" \
"ds_read_b64_tr_b16 a[158:159], v120 offset:15360\n" \
"s_waitcnt lgkmcnt(4)\n" \
"ds_read_b64_tr_b16 a[216:217], v121 offset:12288\n" \
"ds_read_b64_tr_b16 a[218:219], v121 offset:13312\n" \
"ds_read_b64_tr_b16 a[220:221], v121 offset:14336\n" \
"ds_read_b64_tr_b16 a[222:223], v121 offset:15360\n" \
"s_xor_b32 s74, s75, 4\n" \
"s_lshl_b32 s74, s74, 19\n" \
"s_add_u32 s74, s74, s71\n" \
"s_add_u32 m0, s62, 8192\n" \
"s_nop 0\n" \
"buffer_load_dwordx4 v112, s[48:51], s74 offen offset:0 lds\n" \
"buffer_load_dwordx4 v113, s[48:51], s74 offen offset:1024 lds\n" \
"buffer_load_dwordx4 v114, s[48:51], s74 offen offset:2048 lds\n" \
"buffer_load_dwordx4 v115, s[48:51], s74 offen offset:3072 lds\n" \
"s_waitcnt vmcnt(33)\n" \
"ds_read_b64_tr_b16 a[160:161], v120 offset:16384\n" \
"ds_read_b64_tr_b16 a[162:163], v120 offset:17408\n" \
"ds_read_b64_tr_b16 a[164:165], v120 offset:18432\n" \
"ds_read_b64_tr_b16 a[166:167], v120 offset:19456\n" \
"s_waitcnt lgkmcnt(4)\n" \
"ds_read_b64_tr_b16 a[224:225], v121 offset:16384\n" \
"ds_read_b64_tr_b16 a[226:227], v121 offset:17408\n" \
"ds_read_b64_tr_b16 a[228:229], v121 offset:18432\n" \
"ds_read_b64_tr_b16 a[230:231], v121 offset:19456\n" \
"s_xor_b32 s74, s75, 5\n" \
"s_lshl_b32 s74, s74, 19\n" \
"s_add_u32 s74, s74, s71\n" \
"s_add_u32 m0, s62, 12288\n" \
"s_nop 0\n" \
"buffer_load_dwordx4 v112, s[48:51], s74 offen offset:0 lds\n" \
"buffer_load_dwordx4 v113, s[48:51], s74 offen offset:1024 lds\n" \
"buffer_load_dwordx4 v114, s[48:51], s74 offen offset:2048 lds\n" \
"buffer_load_dwordx4 v115, s[48:51], s74 offen offset:3072 lds\n" \
"s_waitcnt vmcnt(33)\n" \
"ds_read_b64_tr_b16 a[168:169], v120 offset:20480\n" \
"ds_read_b64_tr_b16 a[170:171], v120 offset:21504\n" \
"ds_read_b64_tr_b16 a[172:173], v120 offset:22528\n" \
"ds_read_b64_tr_b16 a[174:175], v120 offset:23552\n" \
"s_waitcnt lgkmcnt(4)\n" \
"ds_read_b64_tr_b16 a[232:233], v121 offset:20480\n" \
"ds_read_b64_tr_b16 a[234:235], v121 offset:21504\n" \
"ds_read_b64_tr_b16 a[236:237], v121 offset:22528\n" \
"ds_read_b64_tr_b16 a[238:239], v121 offset:23552\n" \
"s_xor_b32 s74, s75, 6\n" \
"s_lshl_b32 s74, s74, 19\n" \
"s_add_u32 s74, s74, s71\n" \
"s_add_u32 m0, s62, 16384\n" \
"s_nop 0\n" \
"buffer_load_dwordx4 v112, s[48:51], s74 offen offset:0 lds\n" \
"buffer_load_dwordx4 v113, s[48:51], s74 offen offset:1024 lds\n" \
"buffer_load_dwordx4 v114, s[48:51], s74 offen offset:2048 lds\n" \
"buffer_load_dwordx4 v115, s[48:51], s74 offen offset:3072 lds\n" \
"s_waitcnt vmcnt(33)\n" \
"ds_read_b64_tr_b16 a[176:177], v120 offset:24576\n" \
"ds_read_b64_tr_b16 a[178:179], v120 offset:25600\n" \
"ds_read_b64_tr_b16 a[180:181], v120 offset:26624\n" \
"ds_read_b64_tr_b16 a[182:183], v120 offset:27648\n" \
"s_waitcnt lgkmcnt(4)\n" \
"ds_read_b64_tr_b16 a[240:241], v121 offset:24576\n" \
"ds_read_b64_tr_b16 a[242:243], v121 offset:25600\n" \
"ds_read_b64_tr_b16 a[244:245], v121 offset:26624\n" \
"ds_read_b64_tr_b16 a[246:247], v121 offset:27648\n" \
"s_xor_b32 s74, s75, 7\n" \
"s_lshl_b32 s74, s74, 19\n" \
"s_add_u32 s74, s74, s71\n" \
"s_add_u32 m0, s62, 20480\n" \
"s_nop 0\n" \
"buffer_load_dwordx4 v112, s[48:51], s74 offen offset:0 lds\n" \
"buffer_load_dwordx4 v113, s[48:51], s74 offen offset:1024 lds\n" \
"buffer_load_dwordx4 v114, s[48:51], s74 offen offset:2048 lds\n" \
"buffer_load_dwordx4 v115, s[48:51], s74 offen offset:3072 lds\n" \
"s_waitcnt vmcnt(33)\n" \
"ds_read_b64_tr_b16 a[184:185], v120 offset:28672\n" \
"ds_read_b64_tr_b16 a[186:187], v120 offset:29696\n" \
"ds_read_b64_tr_b16 a[188:189], v120 offset:30720\n" \
"ds_read_b64_tr_b16 a[190:191], v120 offset:31744\n" \
"s_waitcnt lgkmcnt(4)\n" \
"ds_read_b64_tr_b16 a[248:249], v121 offset:28672\n" \
"ds_read_b64_tr_b16 a[250:251], v121 offset:29696\n" \
"ds_read_b64_tr_b16 a[252:253], v121 offset:30720\n" \
"ds_read_b64_tr_b16 a[254:255], v121 offset:31744\n" \
"s_mov_b32 s48, s84\n" \
"s_and_b32 s49, s85, 0xffff\n" \
"s_mov_b32 s50, 4194304\n" \
"s_mov_b32 s51, 0x20000\n" \
"s_xor_b32 s74, s75, 0\n" \
"s_lshl_b32 s74, s74, 19\n" \
"s_add_u32 s74, s74, s71\n" \
"s_add_u32 m0, s62, 24576\n" \
"s_nop 0\n" \
"buffer_load_dwordx4 v112, s[48:51], s74 offen offset:0 lds\n" \
"buffer_load_dwordx4 v113, s[48:51], s74 offen offset:1024 lds\n" \
"buffer_load_dwordx4 v114, s[48:51], s74 offen offset:2048 lds\n" \
"buffer_load_dwordx4 v115, s[48:51], s74 offen offset:3072 lds\n" \
"s_waitcnt vmcnt(33)\n" \
"ds_read_b64_tr_b16 v[128:129], v120 offset:32768\n" \
"ds_read_b64_tr_b16 v[130:131], v120 offset:33792\n" \
"ds_read_b64_tr_b16 v[132:133], v120 offset:34816\n" \
"ds_read_b64_tr_b16 v[134:135], v120 offset:35840\n" \
"s_waitcnt lgkmcnt(4)\n" \
"ds_read_b64_tr_b16 v[192:193], v121 offset:32768\n" \
"ds_read_b64_tr_b16 v[194:195], v121 offset:33792\n" \
"ds_read_b64_tr_b16 v[196:197], v121 offset:34816\n" \
"ds_read_b64_tr_b16 v[198:199], v121 offset:35840\n" \
"s_xor_b32 s74, s75, 1\n" \
"s_lshl_b32 s74, s74, 19\n" \
"s_add_u32 s74, s74, s71\n" \
"s_add_u32 m0, s62, 28672\n" \
"s_nop 0\n" \
"buffer_load_dwordx4 v112, s[48:51], s74 offen offset:0 lds\n" \
"buffer_load_dwordx4 v113, s[48:51], s74 offen offset:1024 lds\n" \
"buffer_load_dwordx4 v114, s[48:51], s74 offen offset:2048 lds\n" \
"buffer_load_dwordx4 v115, s[48:51], s74 offen offset:3072 lds\n" \
"s_waitcnt vmcnt(33)\n" \
"ds_read_b64_tr_b16 v[136:137], v120 offset:36864\n" \
"ds_read_b64_tr_b16 v[138:139], v120 offset:37888\n" \
"ds_read_b64_tr_b16 v[140:141], v120 offset:38912\n" \
"ds_read_b64_tr_b16 v[142:143], v120 offset:39936\n" \
"s_waitcnt lgkmcnt(4)\n" \
"ds_read_b64_tr_b16 v[200:201], v121 offset:36864\n" \
"ds_read_b64_tr_b16 v[202:203], v121 offset:37888\n" \
"ds_read_b64_tr_b16 v[204:205], v121 offset:38912\n" \
"ds_read_b64_tr_b16 v[206:207], v121 offset:39936\n" \
"s_xor_b32 s74, s75, 2\n" \
"s_lshl_b32 s74, s74, 19\n" \
"s_add_u32 s74, s74, s71\n" \
"s_add_u32 m0, s62, 32768\n" \
"s_nop 0\n" \
"buffer_load_dwordx4 v112, s[48:51], s74 offen offset:0 lds\n" \
"buffer_load_dwordx4 v113, s[48:51], s74 offen offset:1024 lds\n" \
"buffer_load_dwordx4 v114, s[48:51], s74 offen offset:2048 lds\n" \
"buffer_load_dwordx4 v115, s[48:51], s74 offen offset:3072 lds\n" \
"s_waitcnt vmcnt(32)\n" \
"ds_read_b64_tr_b16 v[144:145], v120 offset:0\n" \
"ds_read_b64_tr_b16 v[146:147], v120 offset:1024\n" \
"ds_read_b64_tr_b16 v[148:149], v120 offset:2048\n" \
"ds_read_b64_tr_b16 v[150:151], v120 offset:3072\n" \
"s_waitcnt lgkmcnt(4)\n" \
"ds_read_b64_tr_b16 v[208:209], v121 offset:0\n" \
"ds_read_b64_tr_b16 v[210:211], v121 offset:1024\n" \
"ds_read_b64_tr_b16 v[212:213], v121 offset:2048\n" \
"ds_read_b64_tr_b16 v[214:215], v121 offset:3072\n" \
"s_xor_b32 s74, s75, 3\n" \
"s_lshl_b32 s74, s74, 19\n" \
"s_add_u32 s74, s74, s71\n" \
"s_add_u32 m0, s62, 36864\n" \
"s_nop 0\n" \
"buffer_load_dwordx4 v112, s[48:51], s74 offen offset:0 lds\n" \
"buffer_load_dwordx4 v113, s[48:51], s74 offen offset:1024 lds\n" \
"buffer_load_dwordx4 v114, s[48:51], s74 offen offset:2048 lds\n" \
"buffer_load_dwordx4 v115, s[48:51], s74 offen offset:3072 lds\n" \
"s_waitcnt vmcnt(32)\n" \
"ds_read_b64_tr_b16 v[152:153], v120 offset:4096\n" \
"ds_read_b64_tr_b16 v[154:155], v120 offset:5120\n" \
"ds_read_b64_tr_b16 v[156:157], v120 offset:6144\n" \
"ds_read_b64_tr_b16 v[158:159], v120 offset:7168\n" \
"s_waitcnt lgkmcnt(4)\n" \
"ds_read_b64_tr_b16 v[216:217], v121 offset:4096\n" \
"ds_read_b64_tr_b16 v[218:219], v121 offset:5120\n" \
"ds_read_b64_tr_b16 v[220:221], v121 offset:6144\n" \
"ds_read_b64_tr_b16 v[222:223], v121 offset:7168\n" \
"s_xor_b32 s74, s75, 4\n" \
"s_lshl_b32 s74, s74, 19\n" \
"s_add_u32 s74, s74, s71\n" \
"s_add_u32 m0, s62, 0\n" \
"s_nop 0\n" \
"buffer_load_dwordx4 v112, s[48:51], s74 offen offset:0 lds\n" \
"buffer_load_dwordx4 v113, s[48:51], s74 offen offset:1024 lds\n" \
"buffer_load_dwordx4 v114, s[48:51], s74 offen offset:2048 lds\n" \
"buffer_load_dwordx4 v115, s[48:51], s74 offen offset:3072 lds\n" \
"s_waitcnt vmcnt(32)\n" \
"ds_read_b64_tr_b16 v[160:161], v120 offset:8192\n" \
"ds_read_b64_tr_b16 v[162:163], v120 offset:9216\n" \
"ds_read_b64_tr_b16 v[164:165], v120 offset:10240\n" \
"ds_read_b64_tr_b16 v[166:167], v120 offset:11264\n" \
"s_waitcnt lgkmcnt(4)\n" \
"ds_read_b64_tr_b16 v[224:225], v121 offset:8192\n" \
"ds_read_b64_tr_b16 v[226:227], v121 offset:9216\n" \
"ds_read_b64_tr_b16 v[228:229], v121 offset:10240\n" \
"ds_read_b64_tr_b16 v[230:231], v121 offset:11264\n" \
"s_xor_b32 s74, s75, 5\n" \
"s_lshl_b32 s74, s74, 19\n" \
"s_add_u32 s74, s74, s71\n" \
"s_add_u32 m0, s62, 4096\n" \
"s_nop 0\n" \
"buffer_load_dwordx4 v112, s[48:51], s74 offen offset:0 lds\n" \
"buffer_load_dwordx4 v113, s[48:51], s74 offen offset:1024 lds\n" \
"buffer_load_dwordx4 v114, s[48:51], s74 offen offset:2048 lds\n" \
"buffer_load_dwordx4 v115, s[48:51], s74 offen offset:3072 lds\n" \
"s_waitcnt vmcnt(32)\n" \
"ds_read_b64_tr_b16 v[168:169], v120 offset:12288\n" \
"ds_read_b64_tr_b16 v[170:171], v120 offset:13312\n" \
"ds_read_b64_tr_b16 v[172:173], v120 offset:14336\n" \
"ds_read_b64_tr_b16 v[174:175], v120 offset:15360\n" \
"s_waitcnt lgkmcnt(4)\n" \
"ds_read_b64_tr_b16 v[232:233], v121 offset:12288\n" \
"ds_read_b64_tr_b16 v[234:235], v121 offset:13312\n" \
"ds_read_b64_tr_b16 v[236:237], v121 offset:14336\n" \
"ds_read_b64_tr_b16 v[238:239], v121 offset:15360\n" \
"s_xor_b32 s74, s75, 6\n" \
"s_lshl_b32 s74, s74, 19\n" \
"s_add_u32 s74, s74, s71\n" \
"s_add_u32 m0, s62, 8192\n" \
"s_nop 0\n" \
"buffer_load_dwordx4 v112, s[48:51], s74 offen offset:0 lds\n" \
"buffer_load_dwordx4 v113, s[48:51], s74 offen offset:1024 lds\n" \
"buffer_load_dwordx4 v114, s[48:51], s74 offen offset:2048 lds\n" \
"buffer_load_dwordx4 v115, s[48:51], s74 offen offset:3072 lds\n" \
"s_waitcnt vmcnt(32)\n" \
"ds_read_b64_tr_b16 v[176:177], v120 offset:16384\n" \
"ds_read_b64_tr_b16 v[178:179], v120 offset:17408\n" \
"ds_read_b64_tr_b16 v[180:181], v120 offset:18432\n" \
"ds_read_b64_tr_b16 v[182:183], v120 offset:19456\n" \
"s_waitcnt lgkmcnt(4)\n" \
"ds_read_b64_tr_b16 v[240:241], v121 offset:16384\n" \
"ds_read_b64_tr_b16 v[242:243], v121 offset:17408\n" \
"ds_read_b64_tr_b16 v[244:245], v121 offset:18432\n" \
"ds_read_b64_tr_b16 v[246:247], v121 offset:19456\n" \
"s_xor_b32 s74, s75, 7\n" \
"s_lshl_b32 s74, s74, 19\n" \
"s_add_u32 s74, s74, s71\n" \
"s_add_u32 m0, s62, 12288\n" \
"s_nop 0\n" \
"buffer_load_dwordx4 v112, s[48:51], s74 offen offset:0 lds\n" \
"buffer_load_dwordx4 v113, s[48:51], s74 offen offset:1024 lds\n" \
"buffer_load_dwordx4 v114, s[48:51], s74 offen offset:2048 lds\n" \
"buffer_load_dwordx4 v115, s[48:51], s74 offen offset:3072 lds\n" \
"s_waitcnt vmcnt(32)\n" \
"ds_read_b64_tr_b16 v[184:185], v120 offset:20480\n" \
"ds_read_b64_tr_b16 v[186:187], v120 offset:21504\n" \
"ds_read_b64_tr_b16 v[188:189], v120 offset:22528\n" \
"ds_read_b64_tr_b16 v[190:191], v120 offset:23552\n" \
"s_waitcnt lgkmcnt(4)\n" \
"ds_read_b64_tr_b16 v[248:249], v121 offset:20480\n" \
"ds_read_b64_tr_b16 v[250:251], v121 offset:21504\n" \
"ds_read_b64_tr_b16 v[252:253], v121 offset:22528\n" \
"ds_read_b64_tr_b16 v[254:255], v121 offset:23552\n" \
"s_mov_b32 s48, s90\n" \
"s_and_b32 s49, s91, 0xffff\n" \
"s_mov_b32 s50, 4194304\n" \
"s_mov_b32 s51, 0x20000\n" \
"s_add_u32 s74, s61, 0\n" \
"s_add_u32 m0, s62, 16384\n" \
"s_nop 0\n" \
"buffer_load_dwordx4 v116, s[48:51], s74 offen offset:0 lds\n" \
"buffer_load_dwordx4 v117, s[48:51], s74 offen offset:1024 lds\n" \
"buffer_load_dwordx4 v118, s[48:51], s74 offen offset:2048 lds\n" \
"buffer_load_dwordx4 v119, s[48:51], s74 offen offset:3072 lds\n" \
"s_waitcnt vmcnt(32)\n" \
"ds_read_b64_tr_b16 a[0:1], v120 offset:24576\n" \
"ds_read_b64_tr_b16 a[2:3], v120 offset:25600\n" \
"ds_read_b64_tr_b16 a[4:5], v120 offset:26624\n" \
"ds_read_b64_tr_b16 a[6:7], v120 offset:27648\n" \
"s_waitcnt lgkmcnt(4)\n" \
"ds_read_b64_tr_b16 a[64:65], v121 offset:24576\n" \
"ds_read_b64_tr_b16 a[66:67], v121 offset:25600\n" \
"ds_read_b64_tr_b16 a[68:69], v121 offset:26624\n" \
"ds_read_b64_tr_b16 a[70:71], v121 offset:27648\n" \
"s_add_u32 s74, s61, 8192\n" \
"s_add_u32 m0, s62, 20480\n" \
"s_nop 0\n" \
"buffer_load_dwordx4 v116, s[48:51], s74 offen offset:0 lds\n" \
"buffer_load_dwordx4 v117, s[48:51], s74 offen offset:1024 lds\n" \
"buffer_load_dwordx4 v118, s[48:51], s74 offen offset:2048 lds\n" \
"buffer_load_dwordx4 v119, s[48:51], s74 offen offset:3072 lds\n" \
"s_waitcnt vmcnt(32)\n" \
"ds_read_b64_tr_b16 a[8:9], v120 offset:28672\n" \
"ds_read_b64_tr_b16 a[10:11], v120 offset:29696\n" \
"ds_read_b64_tr_b16 a[12:13], v120 offset:30720\n" \
"ds_read_b64_tr_b16 a[14:15], v120 offset:31744\n" \
"s_waitcnt lgkmcnt(4)\n" \
"ds_read_b64_tr_b16 a[72:73], v121 offset:28672\n" \
"ds_read_b64_tr_b16 a[74:75], v121 offset:29696\n" \
"ds_read_b64_tr_b16 a[76:77], v121 offset:30720\n" \
"ds_read_b64_tr_b16 a[78:79], v121 offset:31744\n" \
"s_add_u32 s74, s61, 65536\n" \
"s_add_u32 m0, s62, 24576\n" \
"s_nop 0\n" \
"buffer_load_dwordx4 v116, s[48:51], s74 offen offset:0 lds\n" \
"buffer_load_dwordx4 v117, s[48:51], s74 offen offset:1024 lds\n" \
"buffer_load_dwordx4 v118, s[48:51], s74 offen offset:2048 lds\n" \
"buffer_load_dwordx4 v119, s[48:51], s74 offen offset:3072 lds\n" \
"s_waitcnt vmcnt(32)\n" \
"ds_read_b64_tr_b16 a[16:17], v120 offset:32768\n" \
"ds_read_b64_tr_b16 a[18:19], v120 offset:33792\n" \
"ds_read_b64_tr_b16 a[20:21], v120 offset:34816\n" \
"ds_read_b64_tr_b16 a[22:23], v120 offset:35840\n" \
"s_waitcnt lgkmcnt(4)\n" \
"ds_read_b64_tr_b16 a[80:81], v121 offset:32768\n" \
"ds_read_b64_tr_b16 a[82:83], v121 offset:33792\n" \
"ds_read_b64_tr_b16 a[84:85], v121 offset:34816\n" \
"ds_read_b64_tr_b16 a[86:87], v121 offset:35840\n" \
"s_add_u32 s74, s61, 73728\n" \
"s_add_u32 m0, s62, 28672\n" \
"s_nop 0\n" \
"buffer_load_dwordx4 v116, s[48:51], s74 offen offset:0 lds\n" \
"buffer_load_dwordx4 v117, s[48:51], s74 offen offset:1024 lds\n" \
"buffer_load_dwordx4 v118, s[48:51], s74 offen offset:2048 lds\n" \
"buffer_load_dwordx4 v119, s[48:51], s74 offen offset:3072 lds\n" \
"s_waitcnt vmcnt(32)\n" \
"ds_read_b64_tr_b16 a[24:25], v120 offset:36864\n" \
"ds_read_b64_tr_b16 a[26:27], v120 offset:37888\n" \
"ds_read_b64_tr_b16 a[28:29], v120 offset:38912\n" \
"ds_read_b64_tr_b16 a[30:31], v120 offset:39936\n" \
"s_waitcnt lgkmcnt(4)\n" \
"ds_read_b64_tr_b16 a[88:89], v121 offset:36864\n" \
"ds_read_b64_tr_b16 a[90:91], v121 offset:37888\n" \
"ds_read_b64_tr_b16 a[92:93], v121 offset:38912\n" \
"ds_read_b64_tr_b16 a[94:95], v121 offset:39936\n" \
"s_waitcnt vmcnt(28)\n" \
"ds_read_b64_tr_b16 a[32:33], v120 offset:0\n" \
"ds_read_b64_tr_b16 a[34:35], v120 offset:1024\n" \
"ds_read_b64_tr_b16 a[36:37], v120 offset:2048\n" \
"ds_read_b64_tr_b16 a[38:39], v120 offset:3072\n" \
"s_waitcnt lgkmcnt(4)\n" \
"ds_read_b64_tr_b16 a[96:97], v121 offset:0\n" \
"ds_read_b64_tr_b16 a[98:99], v121 offset:1024\n" \
"ds_read_b64_tr_b16 a[100:101], v121 offset:2048\n" \
"ds_read_b64_tr_b16 a[102:103], v121 offset:3072\n" \
"s_waitcnt vmcnt(24)\n" \
"ds_read_b64_tr_b16 a[40:41], v120 offset:4096\n" \
"ds_read_b64_tr_b16 a[42:43], v120 offset:5120\n" \
"ds_read_b64_tr_b16 a[44:45], v120 offset:6144\n" \
"ds_read_b64_tr_b16 a[46:47], v120 offset:7168\n" \
"s_waitcnt lgkmcnt(4)\n" \
"ds_read_b64_tr_b16 a[104:105], v121 offset:4096\n" \
"ds_read_b64_tr_b16 a[106:107], v121 offset:5120\n" \
"ds_read_b64_tr_b16 a[108:109], v121 offset:6144\n" \
"ds_read_b64_tr_b16 a[110:111], v121 offset:7168\n" \
"s_waitcnt vmcnt(20)\n" \
"ds_read_b64_tr_b16 a[48:49], v120 offset:8192\n" \
"ds_read_b64_tr_b16 a[50:51], v120 offset:9216\n" \
"ds_read_b64_tr_b16 a[52:53], v120 offset:10240\n" \
"ds_read_b64_tr_b16 a[54:55], v120 offset:11264\n" \
"s_waitcnt lgkmcnt(4)\n" \
"ds_read_b64_tr_b16 a[112:113], v121 offset:8192\n" \
"ds_read_b64_tr_b16 a[114:115], v121 offset:9216\n" \
"ds_read_b64_tr_b16 a[116:117], v121 offset:10240\n" \
"ds_read_b64_tr_b16 a[118:119], v121 offset:11264\n" \
"s_add_u32 s99, s96, 2719744\n" \
"buffer_load_dword v125, off, s[52:55], s99 sc1\n" \
"s_waitcnt vmcnt(17)\n" \
"ds_read_b64_tr_b16 a[56:57], v120 offset:12288\n" \
"ds_read_b64_tr_b16 a[58:59], v120 offset:13312\n" \
"ds_read_b64_tr_b16 a[60:61], v120 offset:14336\n" \
"ds_read_b64_tr_b16 a[62:63], v120 offset:15360\n" \
"s_waitcnt lgkmcnt(4)\n" \
"ds_read_b64_tr_b16 a[120:121], v121 offset:12288\n" \
"ds_read_b64_tr_b16 a[122:123], v121 offset:13312\n" \
"ds_read_b64_tr_b16 a[124:125], v121 offset:14336\n" \
"ds_read_b64_tr_b16 a[126:127], v121 offset:15360\n" \
"s_waitcnt vmcnt(13)\n" \
"ds_read_b64_tr_b16 v[16:17], v106 offset:16384\n" \
"ds_read_b64_tr_b16 v[32:33], v107 offset:16384\n" \
"ds_read_b64_tr_b16 v[48:49], v108 offset:16384\n" \
"ds_read_b64_tr_b16 v[64:65], v109 offset:16384\n" \
"s_waitcnt lgkmcnt(4)\n" \
"ds_read_b64_tr_b16 v[18:19], v106 offset:18432\n" \
"ds_read_b64_tr_b16 v[34:35], v107 offset:18432\n" \
"ds_read_b64_tr_b16 v[50:51], v108 offset:18432\n" \
"ds_read_b64_tr_b16 v[66:67], v109 offset:18432\n" \
"s_waitcnt vmcnt(9)\n" \
"ds_read_b64_tr_b16 v[20:21], v106 offset:20480\n" \
"ds_read_b64_tr_b16 v[36:37], v107 offset:20480\n" \
"ds_read_b64_tr_b16 v[52:53], v108 offset:20480\n" \
"ds_read_b64_tr_b16 v[68:69], v109 offset:20480\n" \
"s_waitcnt lgkmcnt(4)\n" \
"ds_read_b64_tr_b16 v[22:23], v106 offset:22528\n" \
"ds_read_b64_tr_b16 v[38:39], v107 offset:22528\n" \
"ds_read_b64_tr_b16 v[54:55], v108 offset:22528\n" \
"ds_read_b64_tr_b16 v[70:71], v109 offset:22528\n" \
"s_waitcnt vmcnt(5)\n" \
"ds_read_b64_tr_b16 v[24:25], v106 offset:24576\n" \
"ds_read_b64_tr_b16 v[40:41], v107 offset:24576\n" \
"ds_read_b64_tr_b16 v[56:57], v108 offset:24576\n" \
"ds_read_b64_tr_b16 v[72:73], v109 offset:24576\n" \
"s_waitcnt lgkmcnt(4)\n" \
"ds_read_b64_tr_b16 v[26:27], v106 offset:26624\n" \
"ds_read_b64_tr_b16 v[42:43], v107 offset:26624\n" \
"ds_read_b64_tr_b16 v[58:59], v108 offset:26624\n" \
"ds_read_b64_tr_b16 v[74:75], v109 offset:26624\n" \
"s_waitcnt vmcnt(1)\n" \
"ds_read_b64_tr_b16 v[28:29], v106 offset:28672\n" \
"ds_read_b64_tr_b16 v[44:45], v107 offset:28672\n" \
"ds_read_b64_tr_b16 v[60:61], v108 offset:28672\n" \
"ds_read_b64_tr_b16 v[76:77], v109 offset:28672\n" \
"s_waitcnt lgkmcnt(4)\n" \
"ds_read_b64_tr_b16 v[30:31], v106 offset:30720\n" \
"ds_read_b64_tr_b16 v[46:47], v107 offset:30720\n" \
"ds_read_b64_tr_b16 v[62:63], v108 offset:30720\n" \
"ds_read_b64_tr_b16 v[78:79], v109 offset:30720\n" \
"s_mov_b32 s48, s82\n" \
"s_and_b32 s49, s83, 0xffff\n" \
"s_mov_b32 s50, 8192\n" \
"s_mov_b32 s51, 0x20000\n" \
"s_lshl_b32 s68, s36, 8\n" \
"buffer_load_dword v80, v105, s[48:51], s68 offen offset:0\n" \
"buffer_load_dword v81, v105, s[48:51], s68 offen offset:32\n" \
"buffer_load_dword v82, v105, s[48:51], s68 offen offset:64\n" \
"buffer_load_dword v83, v105, s[48:51], s68 offen offset:96\n" \
"buffer_load_dword v84, v105, s[48:51], s68 offen offset:128\n" \
"buffer_load_dword v85, v105, s[48:51], s68 offen offset:160\n" \
"buffer_load_dword v86, v105, s[48:51], s68 offen offset:192\n" \
"buffer_load_dword v87, v105, s[48:51], s68 offen offset:224\n" \
"s_waitcnt vmcnt(8)\n" \
"s_bfe_u32 s98, %[id], 458760\n" \
"s_bitcmp1_b32 %[id], 15\n" \
"s_cbranch_scc0 .Lch_hn_%=\n" \
"s_waitcnt vmcnt(0)\n" \
"s_branch .Lch_hk_%=\n" \
".Lch_hn_%=:\n" \
"s_mov_b32 s67, 0x40000\n" \
".Lch_hc_%=:\n" \
"v_readfirstlane_b32 s98, v125\n" \
"s_lshr_b32 s99, s98, 8\n" \
"s_cmp_eq_u32 s99, s97\n" \
"s_cbranch_scc1 .Lch_hg_%=\n" \
"s_sleep 2\n" \
"s_add_u32 s99, s96, 2719744\n" \
"buffer_load_dword v125, off, s[52:55], s99 sc1\n" \
"s_waitcnt vmcnt(0)\n" \
"s_sub_u32 s67, s67, 1\n" \
"s_cbranch_scc0 .Lch_hc_%=\n" \
".Lch_hg_%=:\n" \
"v_mov_b32 v125, 0\n" \
"s_add_u32 s99, s96, 2641920\n" \
"s_mov_b64 exec, 1\n" \
"buffer_store_dword v125, off, s[52:55], s99 sc1\n" \
"s_mov_b64 exec, -1\n" \
"s_lshr_b32 s99, %[id], 8\n" \
"s_xor_b32 s99, s99, s98\n" \
"s_and_b32 s99, s99, 0x70\n" \
"s_cbranch_scc1 .Lch_redo_%=\n" \
".Lch_hk_%=:\n" \
"s_bfe_u32 s39, s98, 65536\n" \
"s_bfe_u32 s40, s98, 65537\n" \
"s_bfe_u32 s41, s98, 65538\n" \
"s_bfe_u32 s42, s98, 65539\n" \
"s_and_b32 s68, s36, 1\n" \
"s_xor_b32 s68, s68, s42\n" \
"s_lshl_b32 s69, s68, 14\n" \
"s_lshl_b32 s68, s41, 13\n" \
"s_add_u32 s69, s69, s68\n" \
"s_lshr_b32 s68, s36, 1\n" \
"s_lshl_b32 s68, s68, 17\n" \
"s_add_u32 s69, s69, s68\n" \
"s_lshl_b32 s68, s38, 18\n" \
"s_add_u32 s69, s69, s68\n" \
"s_lshl_b32 s68, s37, 7\n" \
"s_add_u32 s69, s69, s68\n" \
"v_bfe_u32 v127, v126, 2, 2\n" \
"v_mul_u32_u24 v7, 32768, v127\n" \
"v_bfe_u32 v127, v126, 5, 1\n" \
"v_mul_u32_u24 v127, 4096, v127\n" \
"v_add_u32 v7, v7, v127\n" \
"v_bfe_u32 v127, v126, 0, 2\n" \
"v_mul_u32_u24 v127, 16, v127\n" \
"v_add_u32 v7, v7, v127\n" \
"v_bfe_u32 v127, v126, 4, 1\n" \
"v_mul_u32_u24 v127, 64, v127\n" \
"v_add_u32 v7, v7, v127\n" \
"v_add_u32 v7, s69, v7\n" \
"s_lshr_b32 s74, s37, 3\n" \
"s_and_b32 s68, s74, 1\n" \
"s_xor_b32 s68, s68, s42\n" \
"s_lshl_b32 s59, s68, 14\n" \
"s_lshl_b32 s68, s41, 13\n" \
"s_add_u32 s59, s59, s68\n" \
"s_lshr_b32 s68, s74, 1\n" \
"s_lshl_b32 s68, s68, 17\n" \
"s_add_u32 s59, s59, s68\n" \
"s_lshl_b32 s68, s38, 18\n" \
"s_add_u32 s59, s59, s68\n" \
"s_bfe_u32 s68, s37, 0x20001\n" \
"s_lshl_b32 s68, s68, 15\n" \
"s_add_u32 s59, s59, s68\n" \
"s_and_b32 s68, s37, 1\n" \
"s_lshl_b32 s68, s68, 12\n" \
"s_add_u32 s59, s59, s68\n" \
"s_add_u32 s59, s59, 2097152\n" \
"s_lshl_b32 s68, s36, 10\n" \
"s_add_u32 s60, s59, s68\n" \
"s_xor_b32 s60, s60, 1048576\n" \
"s_mov_b32 s58, 2097152\n" \
"s_and_b32 s68, s74, 1\n" \
"s_xor_b32 s72, s68, s40\n" \
"s_lshl_b32 s68, s38, 12\n" \
"s_lshl_b32 s69, s37, 7\n" \
"s_add_u32 s68, s68, s69\n" \
"s_lshl_b32 s69, s39, 13\n" \
"s_add_u32 s68, s68, s69\n" \
"v_bfe_u32 v127, v126, 5, 1\n" \
"v_mul_u32_u24 v104, 16384, v127\n" \
"v_bfe_u32 v127, v126, 1, 2\n" \
"v_mul_u32_u24 v127, 32, v127\n" \
"v_add_u32 v104, v104, v127\n" \
"v_bfe_u32 v127, v126, 4, 1\n" \
"v_mul_u32_u24 v127, 16, v127\n" \
"v_add_u32 v104, v104, v127\n" \
"v_bfe_u32 v127, v126, 0, 1\n" \
"v_mul_u32_u24 v127, 8, v127\n" \
"v_add_u32 v104, v104, v127\n" \
"v_add_u32 v104, s68, v104\n" \
"s_waitcnt lgkmcnt(0)\n" \
"s_barrier\n" \
"ds_write_b128 v6, v[16:19] offset:0\n" \
"ds_write_b128 v6, v[20:23] offset:1024\n" \
"ds_write_b128 v6, v[24:27] offset:2048\n" \
"ds_write_b128 v6, v[28:31] offset:3072\n" \
"ds_write_b128 v6, v[32:35] offset:4096\n" \
"ds_write_b128 v6, v[36:39] offset:5120\n" \
"ds_write_b128 v6, v[40:43] offset:6144\n" \
"ds_write_b128 v6, v[44:47] offset:7168\n" \
"s_waitcnt lgkmcnt(0)\n" \
"ds_write_b128 v6, v[48:51] offset:8192\n" \
"ds_write_b128 v6, v[52:55] offset:9216\n" \
"ds_write_b128 v6, v[56:59] offset:10240\n" \
"ds_write_b128 v6, v[60:63] offset:11264\n" \
"ds_write_b128 v6, v[64:67] offset:12288\n" \
"ds_write_b128 v6, v[68:71] offset:13312\n" \
"ds_write_b128 v6, v[72:75] offset:14336\n" \
"ds_write_b128 v6, v[76:79] offset:15360\n" \
"s_waitcnt lgkmcnt(0)\n" \
"ds_write_b128 v1, v[100:103] offset:49664\n" \
"ds_write_b32 v1, v104 offset:50688\n" \
"s_lshl_b32 s66, s37, 8\n" \
"s_add_u32 s66, s66, 2621440\n" \
"s_lshl_b32 s65, s41, 13\n" \
"s_xor_b32 s66, s66, s65\n" \
"s_mov_b32 s65, 1\n" \
"s_mov_b32 s67, 0x40000\n" \
"s_lshl_b32 s68, s37, 14\n" \
"s_lshl_b32 s69, s36, 12\n" \
"s_add_u32 s61, s68, s69\n" \
"s_add_u32 s61, s61, 8388608\n" \
"s_mov_b32 s62, s69\n" \
"s_mov_b32 s63, 0\n" \
"s_mov_b32 s64, 0\n" \
"s_mov_b32 s70, 1040356334\n" \
"s_mov_b32 s57, 32768\n" \
"s_mov_b32 s73, 1006632960\n" \
"s_waitcnt vmcnt(0)\n" \
"s_mov_b32 s68, 0x05040100\n" \
"s_mov_b32 s69, 0x07060302\n" \
"v_perm_b32 v16, v81, v80, s68\n" \
"v_perm_b32 v17, v83, v82, s68\n" \
"v_perm_b32 v18, v81, v80, s69\n" \
"v_perm_b32 v19, v83, v82, s69\n" \
"v_perm_b32 v20, v85, v84, s68\n" \
"v_perm_b32 v21, v87, v86, s68\n" \
"v_perm_b32 v22, v85, v84, s69\n" \
"v_perm_b32 v23, v87, v86, s69\n" \
".Lch_kv0_%=:\n" \
"buffer_load_dword v115, off, s[52:55], s66 sc1\n" \
"s_waitcnt vmcnt(0)\n" \
"v_readfirstlane_b32 s68, v115\n" \
"s_cmp_ge_i32 s68, s65\n" \
"s_cbranch_scc1 .Lch_kv0ok_%=\n" \
"s_sleep 2\n" \
"s_sub_u32 s67, s67, 1\n" \
"s_cbranch_scc0 .Lch_kv0_%=\n" \
"s_mov_b32 s67, 0\n" \
".Lch_kv0ok_%=:\n" \
"s_add_u32 m0, s62, 8192\n" \
"s_nop 0\n" \
"buffer_load_dwordx4 v1, s[52:55], s61 offen offset:0 sc1 lds\n" \
"buffer_load_dwordx4 v1, s[52:55], s61 offen offset:1024 sc1 lds\n" \
"buffer_load_dwordx4 v1, s[52:55], s61 offen offset:2048 sc1 lds\n" \
"buffer_load_dwordx4 v1, s[52:55], s61 offen offset:3072 sc1 lds\n" \
"s_add_u32 s61, s61, 524288\n" \
"s_xor_b32 s62, s62, 16384\n" \
"s_xor_b32 s66, s66, 128\n" \
"s_mov_b32 s65, 2\n" \
"buffer_load_dword v115, off, s[52:55], s66 sc1\n" \
"s_waitcnt vmcnt(0)\n" \
"s_memrealtime s[50:51]\n" \
"s_waitcnt lgkmcnt(0)\n" \
"s_mul_hi_u32 s68, s50, %[tqm]\n" \
"s_mul_i32 s71, s68, s45\n" \
"v_mov_b32 v120, 0\n" \
"v_mov_b32 v121, 0\n" \
"ds_write_b32 v120, v121 offset:51716\n" \
"s_waitcnt lgkmcnt(0)\n" \
"s_barrier\n" \
"v_add_u32 v120, s63, v1\n" \
"v_add_u32 v121, s63, v3\n" \
"v_mov_b32 v122, 0\n" \
"v_xor_b32 v112, 2048, v3\n" \
"v_xor_b32 v113, 4096, v3\n" \
"v_xor_b32 v114, 6144, v3\n" \
"v_mov_b32 v116, -1\n" \
"v_mov_b32 v117, -1\n" \
"v_mov_b32 v118, -1\n" \
"v_mov_b32 v119, -1\n" \
"ds_read_b128 v[72:75], v120 offset:8192\n" \
"ds_read_b128 v[76:79], v120 offset:9216\n" \
"ds_read_b128 v[80:83], v120 offset:10240\n" \
"ds_read_b128 v[84:87], v120 offset:11264\n" \
"ds_read_b128 v[88:91], v120 offset:12288\n" \
"ds_read_b128 v[92:95], v120 offset:13312\n" \
"ds_read_b128 v[96:99], v120 offset:14336\n" \
"ds_read_b128 v[100:103], v120 offset:15360\n" \
"ds_read_b128 v[104:107], v121 offset:16384\n" \
"ds_read_b128 v[108:111], v121 offset:17408\n" \
"v_mov_b64 v[48:49], 0\n" \
"v_mov_b64 v[50:51], 0\n" \
"v_mov_b64 v[52:53], 0\n" \
"v_mov_b64 v[54:55], 0\n" \
"v_mov_b64 v[56:57], 0\n" \
"v_mov_b64 v[58:59], 0\n" \
"v_mov_b64 v[60:61], 0\n" \
"v_mov_b64 v[62:63], 0\n" \
"v_mov_b64 v[64:65], 0\n" \
"v_mov_b64 v[66:67], 0\n" \
"v_mov_b64 v[68:69], 0\n" \
"v_mov_b64 v[70:71], 0\n" \
"v_readfirstlane_b32 s68, v115\n" \
"s_cmp_ge_i32 s68, s65\n" \
"s_cbranch_scc1 .Lch_pkvgo_%=\n" \
".Lch_pkvwait_%=:\n" \
"s_cmp_ge_u32 s64, 62\n" \
"s_cbranch_scc1 .Lch_pkvgo_%=\n" \
"buffer_load_dword v115, off, s[52:55], s66 sc1\n" \
"s_waitcnt vmcnt(0)\n" \
"v_readfirstlane_b32 s68, v115\n" \
"s_cmp_ge_i32 s68, s65\n" \
"s_cbranch_scc1 .Lch_pkvgo_%=\n" \
"s_sleep 1\n" \
"s_sub_u32 s67, s67, 1\n" \
"s_cbranch_scc0 .Lch_pkvwait_%=\n" \
"s_mov_b32 s67, 0\n" \
".Lch_pkvgo_%=:\n" \
"s_xor_b32 s66, s66, 128\n" \
"s_add_u32 s65, s65, 1\n" \
"s_mov_b32 s42, 0\n" \
"s_waitcnt lgkmcnt(0)\n" \
"s_branch .Lch_entry_%=\n" \
".Lch_top_%=:\n" \
"s_waitcnt vmcnt(1)\n" \
".Lch_entry_%=:\n" \
"ds_write_b128 v3, v[16:19] offset:0\n" \
"v_smfmac_f32_16x16x64_bf16 v[48:51], v[16:19], a[128:135], v0\n" \
"v_smfmac_f32_16x16x64_bf16 v[52:55], v[16:19], a[192:199], v0\n" \
"ds_write_b128 v3, v[20:23] offset:1024\n" \
"v_smfmac_f32_16x16x64_bf16 v[56:59], v[16:19], a[0:7], v0\n" \
"v_max3_u32 v120, v16, v17, v18\n" \
"v_smfmac_f32_16x16x64_bf16 v[60:63], v[16:19], a[64:71], v0\n" \
"v_max3_u32 v120, v120, v19, v20\n" \
"v_smfmac_f32_16x16x64_bf16 v[48:51], v[20:23], a[136:143], v0\n" \
"v_max3_u32 v120, v120, v21, v22\n" \
"v_smfmac_f32_16x16x64_bf16 v[52:55], v[20:23], a[200:207], v0\n" \
"v_max_u32 v120, v120, v23\n" \
"v_smfmac_f32_16x16x64_bf16 v[56:59], v[20:23], a[8:15], v0\n" \
"v_cmp_eq_u32 vcc, -1, v120\n" \
"v_smfmac_f32_16x16x64_bf16 v[60:63], v[20:23], a[72:79], v0\n" \
"s_cbranch_vccnz .Lch_h2retry_%=\n" \
".Lch_h2go_%=:\n" \
"s_waitcnt lgkmcnt(0)\n" \
"s_barrier\n" \
"ds_read_b128 v[24:27], v112 offset:0\n" \
"ds_read_b128 v[28:31], v112 offset:1024\n" \
"v_smfmac_f32_16x16x64_bf16 v[64:67], v[16:19], v[128:135], v0\n" \
"ds_read_b128 v[32:35], v113 offset:0\n" \
"ds_read_b128 v[36:39], v113 offset:1024\n" \
"v_smfmac_f32_16x16x64_bf16 v[68:71], v[16:19], v[192:199], v0\n" \
"ds_read_b128 v[40:43], v114 offset:0\n" \
"ds_read_b128 v[44:47], v114 offset:1024\n" \
"v_smfmac_f32_16x16x64_bf16 v[64:67], v[20:23], v[136:143], v0\n" \
"v_smfmac_f32_16x16x64_bf16 v[68:71], v[20:23], v[200:207], v0\n" \
"s_waitcnt lgkmcnt(5)\n" \
"v_smfmac_f32_16x16x64_bf16 v[48:51], v[24:27], a[144:151], v0\n" \
"s_add_u32 m0, s62, 8192\n" \
"v_smfmac_f32_16x16x64_bf16 v[52:55], v[24:27], a[208:215], v0\n" \
"s_waitcnt lgkmcnt(4)\n" \
"v_smfmac_f32_16x16x64_bf16 v[48:51], v[28:31], a[152:159], v0\n" \
"buffer_load_dwordx4 v1, s[52:55], s61 offen offset:0 sc1 lds\n" \
"v_smfmac_f32_16x16x64_bf16 v[52:55], v[28:31], a[216:223], v0\n" \
"s_waitcnt lgkmcnt(3)\n" \
"v_smfmac_f32_16x16x64_bf16 v[48:51], v[32:35], a[160:167], v0\n" \
"buffer_load_dwordx4 v1, s[52:55], s61 offen offset:1024 sc1 lds\n" \
"v_smfmac_f32_16x16x64_bf16 v[52:55], v[32:35], a[224:231], v0\n" \
"s_waitcnt lgkmcnt(2)\n" \
"v_smfmac_f32_16x16x64_bf16 v[48:51], v[36:39], a[168:175], v0\n" \
"buffer_load_dwordx4 v1, s[52:55], s61 offen offset:2048 sc1 lds\n" \
"v_smfmac_f32_16x16x64_bf16 v[52:55], v[36:39], a[232:239], v0\n" \
"s_waitcnt lgkmcnt(1)\n" \
"v_smfmac_f32_16x16x64_bf16 v[48:51], v[40:43], a[176:183], v0\n" \
"buffer_load_dwordx4 v1, s[52:55], s61 offen offset:3072 sc1 lds\n" \
"v_smfmac_f32_16x16x64_bf16 v[52:55], v[40:43], a[240:247], v0\n" \
"s_waitcnt lgkmcnt(0)\n" \
"ds_read_b128 v[16:19], v6 offset:0\n" \
"v_smfmac_f32_16x16x64_bf16 v[48:51], v[44:47], a[184:191], v0\n" \
"v_smfmac_f32_16x16x64_bf16 v[52:55], v[44:47], a[248:255], v0\n" \
"ds_read_b128 v[20:23], v6 offset:1024\n" \
"v_smfmac_f32_16x16x64_bf16 v[56:59], v[24:27], a[16:23], v0\n" \
"v_add_f32 v48, v48, v50\n" \
"v_smfmac_f32_16x16x64_bf16 v[56:59], v[28:31], a[24:31], v0\n" \
"v_add_f32 v49, v49, v51\n" \
"v_smfmac_f32_16x16x64_bf16 v[56:59], v[32:35], a[32:39], v0\n" \
"s_xor_b32 s60, s60, 1048576\n" \
"v_add_f32 v52, v52, v54\n" \
"v_smfmac_f32_16x16x64_bf16 v[56:59], v[36:39], a[40:47], v0\n" \
"v_add_f32 v53, v53, v55\n" \
"v_smfmac_f32_16x16x64_bf16 v[56:59], v[40:43], a[48:55], v0\n" \
"v_mul_f32 v48, s70, v48\n" \
"v_smfmac_f32_16x16x64_bf16 v[56:59], v[44:47], a[56:63], v0\n" \
"s_add_u32 s64, s64, 1\n" \
"v_mul_f32 v49, s70, v49\n" \
"v_smfmac_f32_16x16x64_bf16 v[60:63], v[24:27], a[80:87], v0\n" \
"s_bfe_u32 s68, s64, 0x10004\n" \
"v_mul_f32 v52, s70, v52\n" \
"v_smfmac_f32_16x16x64_bf16 v[60:63], v[28:31], a[88:95], v0\n" \
"s_xor_b32 s68, s68, s72\n" \
"v_mul_f32 v53, s70, v53\n" \
"v_smfmac_f32_16x16x64_bf16 v[60:63], v[32:35], a[96:103], v0\n" \
"s_lshl_b32 s68, s68, 1\n" \
"v_cvt_pk_bf16_f32 v48, v48, v49\n" \
"v_smfmac_f32_16x16x64_bf16 v[60:63], v[36:39], a[104:111], v0\n" \
"s_or_b32 s68, s68, s39\n" \
"v_cvt_pk_bf16_f32 v52, v52, v53\n" \
"v_smfmac_f32_16x16x64_bf16 v[60:63], v[40:43], a[112:119], v0\n" \
"s_lshl_b32 s68, s68, 13\n" \
"ds_write2_b32 v4, v48, v52 offset0:1 offset1:33\n" \
"v_add_f32 v56, v56, v58\n" \
"v_smfmac_f32_16x16x64_bf16 v[60:63], v[44:47], a[120:127], v0\n" \
"s_add_u32 s56, s57, s68\n" \
"v_add_f32 v57, v57, v59\n" \
"v_smfmac_f32_16x16x64_bf16 v[64:67], v[24:27], v[144:151], v0\n" \
"v_cvt_pk_bf16_f32 v56, v56, v57\n" \
"v_smfmac_f32_16x16x64_bf16 v[68:71], v[24:27], v[208:215], v0\n" \
"v_add_f32 v60, v60, v62\n" \
"v_add_f32 v61, v61, v63\n" \
"v_smfmac_f32_16x16x64_bf16 v[64:67], v[28:31], v[152:159], v0\n" \
"v_cvt_pk_bf16_f32 v60, v60, v61\n" \
"v_smfmac_f32_16x16x64_bf16 v[68:71], v[28:31], v[216:223], v0\n" \
"ds_write2_b32 v4, v56, v60 offset0:0 offset1:32\n" \
"v_smfmac_f32_16x16x64_bf16 v[64:67], v[32:35], v[160:167], v0\n" \
"ds_read_b128 v[24:27], v6 offset:2048\n" \
"v_smfmac_f32_16x16x64_bf16 v[68:71], v[32:35], v[224:231], v0\n" \
"ds_read_b128 v[28:31], v6 offset:3072\n" \
"v_smfmac_f32_16x16x64_bf16 v[64:67], v[36:39], v[168:175], v0\n" \
"ds_read_b128 v[32:35], v6 offset:4096\n" \
"v_smfmac_f32_16x16x64_bf16 v[68:71], v[36:39], v[232:239], v0\n" \
"s_waitcnt lgkmcnt(3)\n" \
"s_barrier\n" \
"ds_read_b64_tr_b16 v[112:113], v5 offset:0\n" \
"ds_read_b64_tr_b16 v[114:115], v5 offset:128\n" \
"v_smfmac_f32_16x16x64_bf16 v[64:67], v[40:43], v[176:183], v0\n" \
"ds_read_b64_tr_b16 v[116:117], v5 offset:256\n" \
"ds_read_b64_tr_b16 v[118:119], v5 offset:384\n" \
"v_smfmac_f32_16x16x64_bf16 v[68:71], v[40:43], v[240:247], v0\n" \
"ds_read_b64_tr_b16 v[120:121], v5 offset:512\n" \
"ds_read_b64_tr_b16 v[122:123], v5 offset:640\n" \
"v_smfmac_f32_16x16x64_bf16 v[64:67], v[44:47], v[184:191], v0\n" \
"ds_read_b64_tr_b16 v[124:125], v5 offset:768\n" \
"ds_read_b64_tr_b16 v[126:127], v5 offset:896\n" \
"v_smfmac_f32_16x16x64_bf16 v[68:71], v[44:47], v[248:255], v0\n" \
"s_waitcnt lgkmcnt(6)\n" \
"v_mfma_f32_16x16x32_bf16 v[48:51], v[72:75], v[112:115], 0\n" \
"s_waitcnt lgkmcnt(4)\n" \
"ds_read_b128 v[36:39], v6 offset:5120\n" \
"v_add_f32 v64, v64, v66\n" \
"v_mfma_f32_16x16x32_bf16 v[48:51], v[76:79], v[116:119], v[48:51]\n" \
"s_waitcnt lgkmcnt(3)\n" \
"ds_read_b128 v[40:43], v6 offset:6144\n" \
"v_add_f32 v65, v65, v67\n" \
"v_mfma_f32_16x16x32_bf16 v[48:51], v[80:83], v[120:123], v[48:51]\n" \
"s_waitcnt lgkmcnt(2)\n" \
"ds_read_b128 v[44:47], v6 offset:7168\n" \
"v_add_f32 v68, v68, v70\n" \
"v_mfma_f32_16x16x32_bf16 v[48:51], v[84:87], v[124:127], v[48:51]\n" \
"v_add_f32 v69, v69, v71\n" \
"v_cvt_pk_bf16_f32 v64, v64, v65\n" \
"v_mfma_f32_16x16x32_bf16 v[52:55], v[88:91], v[112:115], 0\n" \
"ds_read_b128 v[72:75], v6 offset:8192\n" \
"v_cvt_pk_bf16_f32 v65, v68, v69\n" \
"v_mfma_f32_16x16x32_bf16 v[52:55], v[92:95], v[116:119], v[52:55]\n" \
"v_exp_f32 v48, v48\n" \
"v_mfma_f32_16x16x32_bf16 v[52:55], v[96:99], v[120:123], v[52:55]\n" \
"v_exp_f32 v49, v49\n" \
"v_mfma_f32_16x16x32_bf16 v[52:55], v[100:103], v[124:127], v[52:55]\n" \
"v_exp_f32 v50, v50\n" \
"v_mfma_f32_16x16x32_bf16 v[56:59], v[112:115], v[112:115], 0\n" \
"v_exp_f32 v51, v51\n" \
"v_mfma_f32_16x16x32_bf16 v[56:59], v[116:119], v[116:119], v[56:59]\n" \
"v_exp_f32 v52, v52\n" \
"v_mfma_f32_16x16x32_bf16 v[56:59], v[120:123], v[120:123], v[56:59]\n" \
"v_exp_f32 v53, v53\n" \
"v_mfma_f32_16x16x32_bf16 v[56:59], v[124:127], v[124:127], v[56:59]\n" \
"v_exp_f32 v54, v54\n" \
"v_exp_f32 v55, v55\n" \
"v_cvt_pk_bf16_f32 v48, v48, v49\n" \
"v_cvt_pk_bf16_f32 v49, v50, v51\n" \
"v_cvt_pk_bf16_f32 v50, v52, v53\n" \
"v_cvt_pk_bf16_f32 v51, v54, v55\n" \
"s_nop 1\n" \
"v_exp_f32 v58, v58\n" \
"v_mfma_f32_16x16x32_bf16 v[112:115], v[48:51], v[104:107], 0\n" \
"v_exp_f32 v59, v59\n" \
"v_mfma_f32_16x16x32_bf16 v[116:119], v[48:51], v[108:111], 0\n" \
"v_cvt_pk_bf16_f32 v12, v58, v59\n" \
"v_cvt_pk_bf16_f32 v15, v58, v59\n" \
"v_mov_b64 v[104:105], 0\n" \
"v_mov_b64 v[106:107], 0\n" \
"v_mfma_f32_16x16x16_bf16 v[112:115], v[12:13], v[64:65], v[112:115]\n" \
"ds_read_b128 v[76:79], v6 offset:9216\n" \
"v_mov_b64 v[108:109], 0\n" \
"v_mfma_f32_16x16x16_bf16 v[116:119], v[14:15], v[64:65], v[116:119]\n" \
"ds_read_b128 v[80:83], v6 offset:10240\n" \
"v_mov_b64 v[110:111], 0\n" \
"v_mfma_f32_16x16x32_bf16 v[60:63], v[48:51], v[8:11], 0\n" \
"ds_read_b128 v[84:87], v6 offset:11264\n" \
"v_cvt_pk_bf16_f32 v112, v112, v113\n" \
"v_mfma_f32_16x16x16_bf16 v[56:59], v[12:13], v[8:9], 0\n" \
"v_cvt_pk_bf16_f32 v113, v116, v117\n" \
"ds_write_b64 v4, v[112:113] offset:1024\n" \
"v_mov_b64 v[52:53], 0\n" \
"v_mov_b64 v[54:55], 0\n" \
"v_mov_b64 v[48:49], 0\n" \
"v_mov_b64 v[50:51], 0\n" \
"s_nop 0\n" \
"s_nop 0\n" \
"v_add_f32 v60, v60, v56\n" \
"v_add_f32 v61, v61, v57\n" \
"v_subrev_u32 v68, 562036736, v60\n" \
"v_subrev_u32 v69, 562036736, v61\n" \
"v_max_u32 v68, v68, v69\n" \
"v_cmp_le_u32 vcc, s73, v68\n" \
"v_rcp_f32 v66, v60\n" \
"v_rcp_f32 v67, v61\n" \
"s_waitcnt vmcnt(0)\n" \
"s_waitcnt lgkmcnt(0)\n" \
"s_barrier\n" \
"ds_read_b64_tr_b16 v[120:121], v5 offset:1024\n" \
"ds_read_b64_tr_b16 v[122:123], v5 offset:1280\n" \
"ds_read_b64_tr_b16 v[124:125], v5 offset:1536\n" \
"ds_read_b64_tr_b16 v[126:127], v5 offset:1792\n" \
"ds_read_b128 v[88:91], v6 offset:12288\n" \
"ds_read_b128 v[92:95], v6 offset:13312\n" \
"ds_read_b128 v[96:99], v6 offset:14336\n" \
"ds_read_b128 v[100:103], v6 offset:15360\n" \
"s_cbranch_vccnz .Lch_slowmax_%=\n" \
".Lch_maxdone_%=:\n" \
"s_waitcnt lgkmcnt(6)\n" \
"v_smfmac_f32_16x16x64_bf16 v[48:51], v[120:123], v[16:23], v0\n" \
"s_waitcnt lgkmcnt(4)\n" \
"v_smfmac_f32_16x16x64_bf16 v[48:51], v[124:127], v[24:31], v0\n" \
"v_smfmac_f32_16x16x64_bf16 v[52:55], v[120:123], v[32:39], v0\n" \
"v_smfmac_f32_16x16x64_bf16 v[52:55], v[124:127], v[40:47], v0\n" \
"v_add_f32 v48, v48, v50\n" \
"v_smfmac_f32_16x16x64_bf16 v[104:107], v[120:123], v[72:79], v0\n" \
"v_add_f32 v49, v49, v51\n" \
"v_smfmac_f32_16x16x64_bf16 v[104:107], v[124:127], v[80:87], v0\n" \
"s_waitcnt lgkmcnt(2)\n" \
"v_mul_f32 v48, v48, v66\n" \
"v_smfmac_f32_16x16x64_bf16 v[108:111], v[120:123], v[88:95], v0\n" \
"s_waitcnt lgkmcnt(0)\n" \
"v_mul_f32 v49, v49, v67\n" \
"v_smfmac_f32_16x16x64_bf16 v[108:111], v[124:127], v[96:103], v0\n" \
"v_cvt_pk_bf16_f32 v68, v48, v49\n" \
"v_add_f32 v52, v52, v54\n" \
"v_add_f32 v53, v53, v55\n" \
"v_mul_f32 v52, v52, v66\n" \
"v_mul_f32 v53, v53, v67\n" \
"v_cvt_pk_bf16_f32 v69, v52, v53\n" \
"v_pk_add_f32 v[104:105], v[104:105], v[106:107]\n" \
"v_pk_mul_f32 v[104:105], v[104:105], v[66:67]\n" \
"v_pk_add_f32 v[108:109], v[108:109], v[110:111]\n" \
"v_cvt_pk_bf16_f32 v70, v104, v105\n" \
"v_pk_mul_f32 v[108:109], v[108:109], v[66:67]\n" \
"v_cvt_pk_bf16_f32 v71, v108, v109\n" \
"buffer_store_dwordx4 v[68:71], v7, s[52:55], s58 offen sc1\n" \
"s_cmp_lg_u32 s36, 0\n" \
"s_cbranch_scc1 .Lch_bew_%=\n" \
"v_mov_b32 v37, 0\n" \
"ds_read_b32 v38, v37 offset:51716\n" \
"ds_read_b128 v[32:35], v1 offset:49664\n" \
"ds_read_b32 v36, v1 offset:50688\n" \
"buffer_load_dword v115, off, s[52:55], s66 sc1\n" \
"s_xor_b32 s58, s58, 1048576\n" \
"s_xor_b32 s63, s63, 16384\n" \
"s_add_u32 s61, s61, 524288\n" \
"s_xor_b32 s62, s62, 16384\n" \
"s_waitcnt lgkmcnt(2)\n" \
"v_readfirstlane_b32 s68, v38\n" \
"s_add_u32 s71, s71, s68\n" \
"s_sub_u32 s71, s71, s42\n" \
".Lch_h1_%=:\n" \
"s_add_u32 s74, s71, s43\n" \
".Lch_da_%=:\n" \
"s_memrealtime s[50:51]\n" \
"s_waitcnt lgkmcnt(0)\n" \
"s_sub_i32 s75, s74, s50\n" \
"s_cmp_lt_i32 s75, -3\n" \
"s_cbranch_scc1 .Lch_dalate_%=\n" \
"s_mul_i32 s75, s75, s77\n" \
"s_ashr_i32 s75, s75, 8\n" \
"s_max_i32 s75, s75, 0\n" \
"s_min_i32 s68, s75, 64\n" \
"s_sub_u32 s68, 67, s68\n" \
"s_lshl_b32 s68, s68, 2\n" \
"s_getpc_b64 s[50:51]\n" \
"s_add_u32 s50, s50, s68\n" \
"s_addc_u32 s51, s51, 0\n" \
"s_setpc_b64 s[50:51]\n" \
"s_nop 3\n" \
"s_nop 3\n" \
"s_nop 3\n" \
"s_nop 3\n" \
"s_nop 3\n" \
"s_nop 3\n" \
"s_nop 3\n" \
"s_nop 3\n" \
"s_nop 3\n" \
"s_nop 3\n" \
"s_nop 3\n" \
"s_nop 3\n" \
"s_nop 3\n" \
"s_nop 3\n" \
"s_nop 3\n" \
"s_nop 3\n" \
"s_nop 3\n" \
"s_nop 3\n" \
"s_nop 3\n" \
"s_nop 3\n" \
"s_nop 3\n" \
"s_nop 3\n" \
"s_nop 3\n" \
"s_nop 3\n" \
"s_nop 3\n" \
"s_nop 3\n" \
"s_nop 3\n" \
"s_nop 3\n" \
"s_nop 3\n" \
"s_nop 3\n" \
"s_nop 3\n" \
"s_nop 3\n" \
"s_nop 3\n" \
"s_nop 3\n" \
"s_nop 3\n" \
"s_nop 3\n" \
"s_nop 3\n" \
"s_nop 3\n" \
"s_nop 3\n" \
"s_nop 3\n" \
"s_nop 3\n" \
"s_nop 3\n" \
"s_nop 3\n" \
"s_nop 3\n" \
"s_nop 3\n" \
"s_nop 3\n" \
"s_nop 3\n" \
"s_nop 3\n" \
"s_nop 3\n" \
"s_nop 3\n" \
"s_nop 3\n" \
"s_nop 3\n" \
"s_nop 3\n" \
"s_nop 3\n" \
"s_nop 3\n" \
"s_nop 3\n" \
"s_nop 3\n" \
"s_nop 3\n" \
"s_nop 3\n" \
"s_nop 3\n" \
"s_nop 3\n" \
"s_nop 3\n" \
"s_nop 3\n" \
"s_nop 3\n" \
"s_cmp_gt_i32 s75, 64\n" \
"s_cbranch_scc1 .Lch_da_%=\n" \
"buffer_load_dwordx4 v[16:19], v1, s[52:55], s59 offen offset:0 sc1\n" \
"buffer_load_dwordx4 v[20:23], v1, s[52:55], s59 offen offset:1024 sc1\n" \
"buffer_load_dwordx4 v[24:27], v1, s[52:55], s59 offen offset:2048 sc1\n" \
"buffer_load_dwordx4 v[28:31], v1, s[52:55], s59 offen offset:3072 sc1\n" \
"v_add_u32 v120, s63, v1\n" \
"v_add_u32 v121, s63, v3\n" \
"v_mov_b32 v122, 0\n" \
"v_xor_b32 v112, 2048, v3\n" \
"v_xor_b32 v113, 4096, v3\n" \
"v_xor_b32 v114, 6144, v3\n" \
"v_mov_b32 v116, -1\n" \
"v_mov_b32 v117, -1\n" \
"v_mov_b32 v118, -1\n" \
"v_mov_b32 v119, -1\n" \
"ds_read_b128 v[72:75], v120 offset:8192\n" \
"ds_read_b128 v[76:79], v120 offset:9216\n" \
"ds_read_b128 v[80:83], v120 offset:10240\n" \
"ds_read_b128 v[84:87], v120 offset:11264\n" \
"ds_read_b128 v[88:91], v120 offset:12288\n" \
"ds_read_b128 v[92:95], v120 offset:13312\n" \
"ds_read_b128 v[96:99], v120 offset:14336\n" \
"ds_read_b128 v[100:103], v120 offset:15360\n" \
"ds_read_b128 v[104:107], v121 offset:16384\n" \
"ds_read_b128 v[108:111], v121 offset:17408\n" \
"v_mov_b64 v[48:49], 0\n" \
"v_mov_b64 v[50:51], 0\n" \
"v_mov_b64 v[52:53], 0\n" \
"v_mov_b64 v[54:55], 0\n" \
"v_mov_b64 v[56:57], 0\n" \
"v_mov_b64 v[58:59], 0\n" \
"v_mov_b64 v[60:61], 0\n" \
"v_mov_b64 v[62:63], 0\n" \
"v_mov_b64 v[64:65], 0\n" \
"v_mov_b64 v[66:67], 0\n" \
"v_mov_b64 v[68:69], 0\n" \
"v_mov_b64 v[70:71], 0\n" \
"s_waitcnt vmcnt(3)\n" \
"v_mfma_f32_16x16x32_bf16 v[40:43], v[32:35], v[16:19], 0\n" \
"v_max3_u32 v44, v16, v17, v18\n" \
"v_max_u32 v44, v44, v19\n" \
"s_waitcnt vmcnt(2)\n" \
"v_mfma_f32_16x16x32_bf16 v[40:43], v[32:35], v[20:23], v[40:43]\n" \
"v_max3_u32 v44, v44, v20, v21\n" \
"v_max3_u32 v44, v44, v22, v23\n" \
"s_waitcnt vmcnt(1)\n" \
"v_mfma_f32_16x16x32_bf16 v[40:43], v[32:35], v[24:27], v[40:43]\n" \
"v_max3_u32 v44, v44, v24, v25\n" \
"v_max3_u32 v44, v44, v26, v27\n" \
"s_waitcnt vmcnt(0)\n" \
"v_mfma_f32_16x16x32_bf16 v[40:43], v[32:35], v[28:31], v[40:43]\n" \
"v_max3_u32 v44, v44, v28, v29\n" \
"v_max3_u32 v44, v44, v30, v31\n" \
"v_cmp_eq_u32 vcc, -1, v44\n" \
"s_cbranch_vccnz .Lch_h1retry_%=\n" \
".Lch_h1go_%=:\n" \
"s_nop 3\n" \
"v_add_f32_dpp v40, v40, v40 row_ror:8 row_mask:0xf bank_mask:0xf\n" \
"v_add_f32_dpp v41, v41, v41 row_ror:8 row_mask:0xf bank_mask:0xf\n" \
"v_add_f32_dpp v42, v42, v42 row_ror:8 row_mask:0xf bank_mask:0xf\n" \
"v_add_f32_dpp v43, v43, v43 row_ror:8 row_mask:0xf bank_mask:0xf\n" \
"v_cvt_pk_bf16_f32 v40, v40, v41\n" \
"v_cvt_pk_bf16_f32 v41, v42, v43\n" \
"s_mov_b64 exec, s[46:47]\n" \
"buffer_store_dwordx2 v[40:41], v36, s[52:55], s57 offen sc1\n" \
"s_mov_b64 exec, -1\n" \
"v_xor_b32 v42, 8192, v36\n" \
"s_add_u32 s68, s71, s44\n" \
"v_mov_b32 v36, s68\n" \
"ds_write_b64 v37, v[36:37] offset:51712\n" \
"s_waitcnt lgkmcnt(0)\n" \
".Lch_be_%=:\n" \
"s_barrier\n" \
"ds_read_b32 v122, v122 offset:51712\n" \
"s_cmp_ge_u32 s64, 63\n" \
"s_cbranch_scc1 .Lch_end_%=\n" \
"s_add_u32 s57, s57, 32768\n" \
"s_xor_b32 s59, s59, 1048576\n" \
"s_mov_b32 s42, 0\n" \
"s_waitcnt lgkmcnt(0)\n" \
"v_readfirstlane_b32 s71, v122\n" \
".Lch_db_%=:\n" \
"s_memrealtime s[50:51]\n" \
"s_waitcnt lgkmcnt(0)\n" \
"s_sub_i32 s75, s71, s50\n" \
"s_cmp_lt_i32 s75, -3\n" \
"s_cbranch_scc1 .Lch_dblate_%=\n" \
"s_mul_i32 s75, s75, s77\n" \
"s_ashr_i32 s75, s75, 8\n" \
"s_max_i32 s75, s75, 0\n" \
"s_min_i32 s68, s75, 64\n" \
"s_sub_u32 s68, 67, s68\n" \
"s_lshl_b32 s68, s68, 2\n" \
"s_getpc_b64 s[50:51]\n" \
"s_add_u32 s50, s50, s68\n" \
"s_addc_u32 s51, s51, 0\n" \
"s_setpc_b64 s[50:51]\n" \
"s_nop 3\n" \
"s_nop 3\n" \
"s_nop 3\n" \
"s_nop 3\n" \
"s_nop 3\n" \
"s_nop 3\n" \
"s_nop 3\n" \
"s_nop 3\n" \
"s_nop 3\n" \
"s_nop 3\n" \
"s_nop 3\n" \
"s_nop 3\n" \
"s_nop 3\n" \
"s_nop 3\n" \
"s_nop 3\n" \
"s_nop 3\n" \
"s_nop 3\n" \
"s_nop 3\n" \
"s_nop 3\n" \
"s_nop 3\n" \
"s_nop 3\n" \
"s_nop 3\n" \
"s_nop 3\n" \
"s_nop 3\n" \
"s_nop 3\n" \
"s_nop 3\n" \
"s_nop 3\n" \
"s_nop 3\n" \
"s_nop 3\n" \
"s_nop 3\n" \
"s_nop 3\n" \
"s_nop 3\n" \
"s_nop 3\n" \
"s_nop 3\n" \
"s_nop 3\n" \
"s_nop 3\n" \
"s_nop 3\n" \
"s_nop 3\n" \
"s_nop 3\n" \
"s_nop 3\n" \
"s_nop 3\n" \
"s_nop 3\n" \
"s_nop 3\n" \
"s_nop 3\n" \
"s_nop 3\n" \
"s_nop 3\n" \
"s_nop 3\n" \
"s_nop 3\n" \
"s_nop 3\n" \
"s_nop 3\n" \
"s_nop 3\n" \
"s_nop 3\n" \
"s_nop 3\n" \
"s_nop 3\n" \
"s_nop 3\n" \
"s_nop 3\n" \
"s_nop 3\n" \
"s_nop 3\n" \
"s_nop 3\n" \
"s_nop 3\n" \
"s_nop 3\n" \
"s_nop 3\n" \
"s_nop 3\n" \
"s_nop 3\n" \
"s_cmp_gt_i32 s75, 64\n" \
"s_cbranch_scc1 .Lch_db_%=\n" \
"buffer_load_dwordx4 v[16:19], v3, s[52:55], s56 offen sc1\n" \
"buffer_load_dwordx4 v[20:23], v3, s[52:55], s56 offen offset:1024 sc1\n" \
"v_readfirstlane_b32 s68, v115\n" \
"s_cmp_ge_i32 s68, s65\n" \
"s_cbranch_scc1 .Lch_lkvgo_%=\n" \
".Lch_lkvwait_%=:\n" \
"s_cmp_ge_u32 s64, 62\n" \
"s_cbranch_scc1 .Lch_lkvgo_%=\n" \
"buffer_load_dword v115, off, s[52:55], s66 sc1\n" \
"s_waitcnt vmcnt(0)\n" \
"v_readfirstlane_b32 s68, v115\n" \
"s_cmp_ge_i32 s68, s65\n" \
"s_cbranch_scc1 .Lch_lkvgo_%=\n" \
"s_sleep 1\n" \
"s_sub_u32 s67, s67, 1\n" \
"s_cbranch_scc0 .Lch_lkvwait_%=\n" \
"s_mov_b32 s67, 0\n" \
".Lch_lkvgo_%=:\n" \
"s_xor_b32 s66, s66, 128\n" \
"s_add_u32 s65, s65, 1\n" \
"buffer_store_dwordx4 v[116:119], v1, s[52:55], s60 offen sc1\n" \
"s_cmp_lg_u32 s36, 0\n" \
"s_cbranch_scc1 .Lch_top_%=\n" \
"s_sub_u32 s74, s57, 32768\n" \
"s_mov_b64 exec, s[46:47]\n" \
"buffer_store_dwordx2 v[40:41], v42, s[52:55], s74 offen sc1\n" \
"s_mov_b64 exec, -1\n" \
"s_waitcnt vmcnt(2)\n" \
"s_branch .Lch_entry_%=\n" \
".Lch_dblate_%=:\n" \
"s_add_u32 s71, s71, s45\n" \
"s_add_u32 s42, s42, s45\n" \
"v_mov_b32 v120, 0\n" \
"v_mov_b32 v121, s42\n" \
"ds_max_u32 v120, v121 offset:51716\n" \
"s_add_u32 s76, s76, 1\n" \
"s_branch .Lch_db_%=\n" \
".Lch_dalate_%=:\n" \
"s_add_u32 s71, s71, s45\n" \
"s_add_u32 s76, s76, 65536\n" \
"s_branch .Lch_h1_%=\n" \
".Lch_bew_%=:\n" \
"buffer_load_dword v115, off, s[52:55], s66 sc1\n" \
"s_xor_b32 s58, s58, 1048576\n" \
"s_xor_b32 s63, s63, 16384\n" \
"s_add_u32 s61, s61, 524288\n" \
"s_xor_b32 s62, s62, 16384\n" \
"v_add_u32 v120, s63, v1\n" \
"v_add_u32 v121, s63, v3\n" \
"v_mov_b32 v122, 0\n" \
"v_xor_b32 v112, 2048, v3\n" \
"v_xor_b32 v113, 4096, v3\n" \
"v_xor_b32 v114, 6144, v3\n" \
"v_mov_b32 v116, -1\n" \
"v_mov_b32 v117, -1\n" \
"v_mov_b32 v118, -1\n" \
"v_mov_b32 v119, -1\n" \
"ds_read_b128 v[72:75], v120 offset:8192\n" \
"ds_read_b128 v[76:79], v120 offset:9216\n" \
"ds_read_b128 v[80:83], v120 offset:10240\n" \
"ds_read_b128 v[84:87], v120 offset:11264\n" \
"ds_read_b128 v[88:91], v120 offset:12288\n" \
"ds_read_b128 v[92:95], v120 offset:13312\n" \
"ds_read_b128 v[96:99], v120 offset:14336\n" \
"ds_read_b128 v[100:103], v120 offset:15360\n" \
"ds_read_b128 v[104:107], v121 offset:16384\n" \
"ds_read_b128 v[108:111], v121 offset:17408\n" \
"v_mov_b64 v[48:49], 0\n" \
"v_mov_b64 v[50:51], 0\n" \
"v_mov_b64 v[52:53], 0\n" \
"v_mov_b64 v[54:55], 0\n" \
"v_mov_b64 v[56:57], 0\n" \
"v_mov_b64 v[58:59], 0\n" \
"v_mov_b64 v[60:61], 0\n" \
"v_mov_b64 v[62:63], 0\n" \
"v_mov_b64 v[64:65], 0\n" \
"v_mov_b64 v[66:67], 0\n" \
"v_mov_b64 v[68:69], 0\n" \
"v_mov_b64 v[70:71], 0\n" \
"s_branch .Lch_be_%=\n" \
".Lch_h2retry_%=:\n" \
"s_add_u32 s76, s76, 1\n" \
"s_sub_u32 s67, s67, 1\n" \
"s_cbranch_scc0 .Lch_h2again_%=\n" \
"s_mov_b32 s67, 0\n" \
"s_branch .Lch_h2go_%=\n" \
".Lch_h2again_%=:\n" \
"s_add_u32 s71, s71, s45\n" \
"s_add_u32 s42, s42, s45\n" \
".Lch_dr_%=:\n" \
"s_memrealtime s[50:51]\n" \
"s_waitcnt lgkmcnt(0)\n" \
"s_sub_i32 s75, s71, s50\n" \
"s_cmp_lt_i32 s75, -6\n" \
"s_cbranch_scc1 .Lch_h2again_%=\n" \
"s_mul_i32 s75, s75, s77\n" \
"s_ashr_i32 s75, s75, 8\n" \
"s_max_i32 s75, s75, 0\n" \
"s_min_i32 s68, s75, 64\n" \
"s_sub_u32 s68, 67, s68\n" \
"s_lshl_b32 s68, s68, 2\n" \
"s_getpc_b64 s[50:51]\n" \
"s_add_u32 s50, s50, s68\n" \
"s_addc_u32 s51, s51, 0\n" \
"s_setpc_b64 s[50:51]\n" \
"s_nop 3\n" \
"s_nop 3\n" \
"s_nop 3\n" \
"s_nop 3\n" \
"s_nop 3\n" \
"s_nop 3\n" \
"s_nop 3\n" \
"s_nop 3\n" \
"s_nop 3\n" \
"s_nop 3\n" \
"s_nop 3\n" \
"s_nop 3\n" \
"s_nop 3\n" \
"s_nop 3\n" \
"s_nop 3\n" \
"s_nop 3\n" \
"s_nop 3\n" \
"s_nop 3\n" \
"s_nop 3\n" \
"s_nop 3\n" \
"s_nop 3\n" \
"s_nop 3\n" \
"s_nop 3\n" \
"s_nop 3\n" \
"s_nop 3\n" \
"s_nop 3\n" \
"s_nop 3\n" \
"s_nop 3\n" \
"s_nop 3\n" \
"s_nop 3\n" \
"s_nop 3\n" \
"s_nop 3\n" \
"s_nop 3\n" \
"s_nop 3\n" \
"s_nop 3\n" \
"s_nop 3\n" \
"s_nop 3\n" \
"s_nop 3\n" \
"s_nop 3\n" \
"s_nop 3\n" \
"s_nop 3\n" \
"s_nop 3\n" \
"s_nop 3\n" \
"s_nop 3\n" \
"s_nop 3\n" \
"s_nop 3\n" \
"s_nop 3\n" \
"s_nop 3\n" \
"s_nop 3\n" \
"s_nop 3\n" \
"s_nop 3\n" \
"s_nop 3\n" \
"s_nop 3\n" \
"s_nop 3\n" \
"s_nop 3\n" \
"s_nop 3\n" \
"s_nop 3\n" \
"s_nop 3\n" \
"s_nop 3\n" \
"s_nop 3\n" \
"s_nop 3\n" \
"s_nop 3\n" \
"s_nop 3\n" \
"s_nop 3\n" \
"s_cmp_gt_i32 s75, 64\n" \
"s_cbranch_scc1 .Lch_dr_%=\n" \
"buffer_load_dwordx4 v[16:19], v3, s[52:55], s56 offen sc1\n" \
"buffer_load_dwordx4 v[20:23], v3, s[52:55], s56 offen offset:1024 sc1\n" \
"v_mov_b32 v120, 0\n" \
"v_mov_b32 v121, s42\n" \
"ds_max_u32 v120, v121 offset:51716\n" \
"v_mov_b64 v[48:49], 0\n" \
"v_mov_b64 v[50:51], 0\n" \
"v_mov_b64 v[52:53], 0\n" \
"v_mov_b64 v[54:55], 0\n" \
"v_mov_b64 v[56:57], 0\n" \
"v_mov_b64 v[58:59], 0\n" \
"v_mov_b64 v[60:61], 0\n" \
"v_mov_b64 v[62:63], 0\n" \
"v_mov_b64 v[64:65], 0\n" \
"v_mov_b64 v[66:67], 0\n" \
"v_mov_b64 v[68:69], 0\n" \
"v_mov_b64 v[70:71], 0\n" \
"s_waitcnt vmcnt(0) lgkmcnt(0)\n" \
"s_branch .Lch_entry_%=\n" \
".Lch_h1retry_%=:\n" \
"s_nop 7\n" \
"s_add_u32 s76, s76, 65536\n" \
"s_add_u32 s71, s71, s45\n" \
"s_sub_u32 s67, s67, 1\n" \
"s_cbranch_scc0 .Lch_h1_%=\n" \
"s_mov_b32 s67, 0\n" \
"s_branch .Lch_h1go_%=\n" \
".Lch_slowmax_%=:\n" \
"s_waitcnt lgkmcnt(0)\n" \
"s_barrier\n" \
"v_add_u32 v68, s63, v1\n" \
"v_add_u32 v69, s63, v3\n" \
"ds_read_b128 v[72:75], v68 offset:8192\n" \
"ds_read_b128 v[76:79], v68 offset:9216\n" \
"ds_read_b128 v[80:83], v68 offset:10240\n" \
"ds_read_b128 v[84:87], v68 offset:11264\n" \
"ds_read_b128 v[88:91], v68 offset:12288\n" \
"ds_read_b128 v[92:95], v68 offset:13312\n" \
"ds_read_b128 v[96:99], v68 offset:14336\n" \
"ds_read_b128 v[100:103], v68 offset:15360\n" \
"ds_read_b128 v[104:107], v69 offset:16384\n" \
"ds_read_b128 v[108:111], v69 offset:17408\n" \
"s_waitcnt lgkmcnt(0)\n" \
"ds_read_b64_tr_b16 v[112:113], v5 offset:0\n" \
"ds_read_b64_tr_b16 v[114:115], v5 offset:128\n" \
"ds_read_b64_tr_b16 v[116:117], v5 offset:256\n" \
"ds_read_b64_tr_b16 v[118:119], v5 offset:384\n" \
"ds_read_b64_tr_b16 v[120:121], v5 offset:512\n" \
"ds_read_b64_tr_b16 v[122:123], v5 offset:640\n" \
"ds_read_b64_tr_b16 v[124:125], v5 offset:768\n" \
"ds_read_b64_tr_b16 v[126:127], v5 offset:896\n" \
"s_waitcnt lgkmcnt(0)\n" \
"v_mfma_f32_16x16x32_bf16 v[48:51], v[72:75], v[112:115], 0\n" \
"v_mfma_f32_16x16x32_bf16 v[48:51], v[76:79], v[116:119], v[48:51]\n" \
"v_mfma_f32_16x16x32_bf16 v[48:51], v[80:83], v[120:123], v[48:51]\n" \
"v_mfma_f32_16x16x32_bf16 v[48:51], v[84:87], v[124:127], v[48:51]\n" \
"v_mfma_f32_16x16x32_bf16 v[52:55], v[88:91], v[112:115], 0\n" \
"v_mfma_f32_16x16x32_bf16 v[52:55], v[92:95], v[116:119], v[52:55]\n" \
"v_mfma_f32_16x16x32_bf16 v[52:55], v[96:99], v[120:123], v[52:55]\n" \
"v_mfma_f32_16x16x32_bf16 v[52:55], v[100:103], v[124:127], v[52:55]\n" \
"v_mfma_f32_16x16x32_bf16 v[56:59], v[112:115], v[112:115], 0\n" \
"v_mfma_f32_16x16x32_bf16 v[56:59], v[116:119], v[116:119], v[56:59]\n" \
"v_mfma_f32_16x16x32_bf16 v[56:59], v[120:123], v[120:123], v[56:59]\n" \
"v_mfma_f32_16x16x32_bf16 v[56:59], v[124:127], v[124:127], v[56:59]\n" \
"s_nop 7\n" \
"ds_read_b128 v[72:75], v6 offset:8192\n" \
"ds_read_b128 v[76:79], v6 offset:9216\n" \
"ds_read_b128 v[80:83], v6 offset:10240\n" \
"ds_read_b128 v[84:87], v6 offset:11264\n" \
"ds_read_b128 v[88:91], v6 offset:12288\n" \
"ds_read_b128 v[92:95], v6 offset:13312\n" \
"ds_read_b128 v[96:99], v6 offset:14336\n" \
"ds_read_b128 v[100:103], v6 offset:15360\n" \
"v_max3_f32 v68, v48, v49, v50\n" \
"v_max3_f32 v68, v68, v51, v52\n" \
"v_max3_f32 v68, v68, v53, v54\n" \
"v_max3_f32 v68, v68, v55, v58\n" \
"v_max_f32 v68, v68, v59\n" \
"v_mov_b32 v69, v68\n" \
"s_nop 1\n" \
"v_permlane32_swap_b32 v68, v69\n" \
"s_nop 1\n" \
"v_max_f32 v68, v68, v69\n" \
"v_mov_b32 v69, v68\n" \
"s_nop 1\n" \
"v_permlane16_swap_b32 v68, v69\n" \
"s_nop 1\n" \
"v_max_f32 v68, v68, v69\n" \
"v_sub_f32 v48, v48, v68\n" \
"v_exp_f32 v48, v48\n" \
"v_sub_f32 v49, v49, v68\n" \
"v_exp_f32 v49, v49\n" \
"v_sub_f32 v50, v50, v68\n" \
"v_exp_f32 v50, v50\n" \
"v_sub_f32 v51, v51, v68\n" \
"v_exp_f32 v51, v51\n" \
"v_sub_f32 v52, v52, v68\n" \
"v_exp_f32 v52, v52\n" \
"v_sub_f32 v53, v53, v68\n" \
"v_exp_f32 v53, v53\n" \
"v_sub_f32 v54, v54, v68\n" \
"v_exp_f32 v54, v54\n" \
"v_sub_f32 v55, v55, v68\n" \
"v_exp_f32 v55, v55\n" \
"v_sub_f32 v58, v58, v68\n" \
"v_exp_f32 v58, v58\n" \
"v_sub_f32 v59, v59, v68\n" \
"v_exp_f32 v59, v59\n" \
"s_nop 0\n" \
"v_cvt_pk_bf16_f32 v48, v48, v49\n" \
"v_cvt_pk_bf16_f32 v49, v50, v51\n" \
"v_cvt_pk_bf16_f32 v50, v52, v53\n" \
"v_cvt_pk_bf16_f32 v51, v54, v55\n" \
"v_cvt_pk_bf16_f32 v12, v58, v59\n" \
"v_cvt_pk_bf16_f32 v15, v58, v59\n" \
"s_nop 1\n" \
"v_mfma_f32_16x16x32_bf16 v[112:115], v[48:51], v[104:107], 0\n" \
"v_mfma_f32_16x16x32_bf16 v[116:119], v[48:51], v[108:111], 0\n" \
"v_mfma_f32_16x16x16_bf16 v[112:115], v[12:13], v[64:65], v[112:115]\n" \
"v_mfma_f32_16x16x16_bf16 v[116:119], v[14:15], v[64:65], v[116:119]\n" \
"v_mfma_f32_16x16x32_bf16 v[60:63], v[48:51], v[8:11], 0\n" \
"v_mfma_f32_16x16x16_bf16 v[56:59], v[12:13], v[8:9], 0\n" \
"s_nop 7\n" \
"v_cvt_pk_bf16_f32 v112, v112, v113\n" \
"v_cvt_pk_bf16_f32 v113, v116, v117\n" \
"ds_write_b64 v4, v[112:113] offset:1024\n" \
"v_add_f32 v60, v60, v56\n" \
"v_add_f32 v61, v61, v57\n" \
"v_rcp_f32 v66, v60\n" \
"v_rcp_f32 v67, v61\n" \
"v_mov_b64 v[48:49], 0\n" \
"v_mov_b64 v[50:51], 0\n" \
"v_mov_b64 v[52:53], 0\n" \
"v_mov_b64 v[54:55], 0\n" \
"v_mov_b64 v[104:105], 0\n" \
"v_mov_b64 v[106:107], 0\n" \
"v_mov_b64 v[108:109], 0\n" \
"v_mov_b64 v[110:111], 0\n" \
"s_waitcnt lgkmcnt(0)\n" \
"s_barrier\n" \
"ds_read_b64_tr_b16 v[120:121], v5 offset:1024\n" \
"ds_read_b64_tr_b16 v[122:123], v5 offset:1280\n" \
"ds_read_b64_tr_b16 v[124:125], v5 offset:1536\n" \
"ds_read_b64_tr_b16 v[126:127], v5 offset:1792\n" \
"s_waitcnt lgkmcnt(0)\n" \
"s_branch .Lch_maxdone_%=\n" \
".Lch_end_%=:\n" \
"s_cmp_lg_u32 s36, 0\n" \
"s_cbranch_scc1 .Lch_exit_%=\n" \
"s_mov_b32 s74, s57\n" \
"s_mov_b64 exec, s[46:47]\n" \
"buffer_store_dwordx2 v[40:41], v42, s[52:55], s74 offen sc1\n" \
"s_mov_b64 exec, -1\n" \
".Lch_exit_%=:\n" \
"s_mov_b32 %[redo], 0\n" \
"s_branch .Lch_out_%=\n" \
".Lch_redo_%=:\n" \
"s_waitcnt vmcnt(0) lgkmcnt(0)\n" \
"s_barrier\n" \
"s_and_b32 s98, s98, 0x7f\n" \
"s_or_b32 %[redo], s98, 0x80\n" \
".Lch_out_%=:\n"
#define CHAIN_CLOBBERS "v0", "v1", "v2", "v3", "v4", "v5", "v6", "v7", "v8", "v9", "v10", "v11", "v12", "v13", "v14", "v15", "v16", "v17", "v18", "v19", "v20", "v21", "v22", "v23", "v24", "v25", "v26", "v27", "v28", "v29", "v30", "v31", "v32", "v33", "v34", "v35", "v36", "v37", "v38", "v39", "v40", "v41", "v42", "v43", "v44", "v45", "v46", "v47", "v48", "v49", "v50", "v51", "v52", "v53", "v54", "v55", "v56", "v57", "v58", "v59", "v60", "v61", "v62", "v63", "v64", "v65", "v66", "v67", "v68", "v69", "v70", "v71", "v72", "v73", "v74", "v75", "v76", "v77", "v78", "v79", "v80", "v81", "v82", "v83", "v84", "v85", "v86", "v87", "v88", "v89", "v90", "v91", "v92", "v93", "v94", "v95", "v96", "v97", "v98", "v99", "v100", "v101", "v102", "v103", "v104", "v105", "v106", "v107", "v108", "v109", "v110", "v111", "v112", "v113", "v114", "v115", "v116", "v117", "v118", "v119", "v120", "v121", "v122", "v123", "v124", "v125", "v126", "v127", "v128", "v129", "v130", "v131", "v132", "v133", "v134", "v135", "v136", "v137", "v138", "v139", "v140", "v141", "v142", "v143", "v144", "v145", "v146", "v147", "v148", "v149", "v150", "v151", "v152", "v153", "v154", "v155", "v156", "v157", "v158", "v159", "v160", "v161", "v162", "v163", "v164", "v165", "v166", "v167", "v168", "v169", "v170", "v171", "v172", "v173", "v174", "v175", "v176", "v177", "v178", "v179", "v180", "v181", "v182", "v183", "v184", "v185", "v186", "v187", "v188", "v189", "v190", "v191", "v192", "v193", "v194", "v195", "v196", "v197", "v198", "v199", "v200", "v201", "v202", "v203", "v204", "v205", "v206", "v207", "v208", "v209", "v210", "v211", "v212", "v213", "v214", "v215", "v216", "v217", "v218", "v219", "v220", "v221", "v222", "v223", "v224", "v225", "v226", "v227", "v228", "v229", "v230", "v231", "v232", "v233", "v234", "v235", "v236", "v237", "v238", "v239", "v240", "v241", "v242", "v243", "v244", "v245", "v246", "v247", "v248", "v249", "v250", "v251", "v252", "v253", "v254", "v255", "a0", "a1", "a2", "a3", "a4", "a5", "a6", "a7", "a8", "a9", "a10", "a11", "a12", "a13", "a14", "a15", "a16", "a17", "a18", "a19", "a20", "a21", "a22", "a23", "a24", "a25", "a26", "a27", "a28", "a29", "a30", "a31", "a32", "a33", "a34", "a35", "a36", "a37", "a38", "a39", "a40", "a41", "a42", "a43", "a44", "a45", "a46", "a47", "a48", "a49", "a50", "a51", "a52", "a53", "a54", "a55", "a56", "a57", "a58", "a59", "a60", "a61", "a62", "a63", "a64", "a65", "a66", "a67", "a68", "a69", "a70", "a71", "a72", "a73", "a74", "a75", "a76", "a77", "a78", "a79", "a80", "a81", "a82", "a83", "a84", "a85", "a86", "a87", "a88", "a89", "a90", "a91", "a92", "a93", "a94", "a95", "a96", "a97", "a98", "a99", "a100", "a101", "a102", "a103", "a104", "a105", "a106", "a107", "a108", "a109", "a110", "a111", "a112", "a113", "a114", "a115", "a116", "a117", "a118", "a119", "a120", "a121", "a122", "a123", "a124", "a125", "a126", "a127", "a128", "a129", "a130", "a131", "a132", "a133", "a134", "a135", "a136", "a137", "a138", "a139", "a140", "a141", "a142", "a143", "a144", "a145", "a146", "a147", "a148", "a149", "a150", "a151", "a152", "a153", "a154", "a155", "a156", "a157", "a158", "a159", "a160", "a161", "a162", "a163", "a164", "a165", "a166", "a167", "a168", "a169", "a170", "a171", "a172", "a173", "a174", "a175", "a176", "a177", "a178", "a179", "a180", "a181", "a182", "a183", "a184", "a185", "a186", "a187", "a188", "a189", "a190", "a191", "a192", "a193", "a194", "a195", "a196", "a197", "a198", "a199", "a200", "a201", "a202", "a203", "a204", "a205", "a206", "a207", "a208", "a209", "a210", "a211", "a212", "a213", "a214", "a215", "a216", "a217", "a218", "a219", "a220", "a221", "a222", "a223", "a224", "a225", "a226", "a227", "a228", "a229", "a230", "a231", "a232", "a233", "a234", "a235", "a236", "a237", "a238", "a239", "a240", "a241", "a242", "a243", "a244", "a245", "a246", "a247", "a248", "a249", "a250", "a251", "a252", "a253", "a254", "a255", "s36", "s37", "s38", "s39", "s40", "s41", "s42", "s43", "s44", "s45", "s46", "s47", "s48", "s49", "s50", "s51", "s52", "s53", "s54", "s55", "s56", "s57", "s58", "s59", "s60", "s61", "s62", "s63", "s64", "s65", "s66", "s67", "s68", "s69", "s70", "s71", "s72", "s73", "s74", "s75", "s76", "s77", "s78", "s79", "s80", "s81", "s82", "s83", "s84", "s85", "s86", "s87", "s88", "s89", "s90", "s91", "s92", "s93", "s94", "s95", "s96", "s97", "s98", "s99", "vcc", "scc", "m0", "memory"

#define XROLE_ASM \
"s_load_dwordx16 s[80:95], %[ka], 72\n" \
"s_waitcnt lgkmcnt(0)\n" \
"s_mov_b32 s64, s94\n" \
"s_and_b32 s65, s95, 0xffff\n" \
"s_mov_b32 s66, 67108864\n" \
"s_mov_b32 s67, 0x20000\n" \
"s_bfe_u32 s96, %[id], 524304\n" \
"s_lshl_b32 s96, s96, 5\n" \
"s_bfe_u32 s99, %[id], 131072\n" \
"s_add_u32 s96, s96, s99\n" \
"s_lshl_b32 s96, s96, 2\n" \
"s_add_u32 s99, s96, 2719744\n" \
"buffer_load_dword v123, off, s[64:67], s99 sc1\n" \
"s_bfe_u32 s36, %[id], 131072\n" \
"s_bfe_u32 s37, %[id], 327682\n" \
"s_bfe_u32 s38, %[id], 65543\n" \
"v_mbcnt_lo_u32_b32 v126, -1, 0\n" \
"v_mbcnt_hi_u32_b32 v126, -1, v126\n" \
"s_mov_b32 s68, s80\n" \
"s_and_b32 s69, s81, 0xffff\n" \
"s_mov_b32 s70, 2097152\n" \
"s_mov_b32 s71, 0x20000\n" \
"s_and_b32 s59, s38, 1\n" \
"s_lshr_b32 s43, s36, 1\n" \
"s_lshl_b32 s43, s43, 14\n" \
"s_xor_b32 s44, s36, s59\n" \
"s_and_b32 s44, s44, 1\n" \
"s_lshl_b32 s44, s44, 9\n" \
"s_add_u32 s43, s43, s44\n" \
"v_bfe_u32 v127, v126, 2, 4\n" \
"v_mul_u32_u24 v1, 1024, v127\n" \
"v_bfe_u32 v127, v126, 0, 2\n" \
"v_mul_u32_u24 v127, 16, v127\n" \
"v_add_u32 v1, v1, v127\n" \
"v_add_u32 v1, s43, v1\n" \
"v_and_b32 v127, 16, v126\n" \
"v_lshlrev_b32 v127, 1, v127\n" \
"v_xor_b32 v1, v1, v127\n" \
"s_lshl_b32 s57, s36, 13\n" \
"s_lshl_b32 s50, s38, 15\n" \
"s_add_u32 m0, s57, 0\n" \
"s_nop 0\n" \
"buffer_load_dwordx4 v1, s[68:71], s50 offen offset:0 lds\n" \
"s_add_u32 m0, s57, 960\n" \
"s_nop 0\n" \
"buffer_load_dwordx4 v1, s[68:71], s50 offen offset:64 lds\n" \
"s_add_u32 m0, s57, 1920\n" \
"s_nop 0\n" \
"buffer_load_dwordx4 v1, s[68:71], s50 offen offset:128 lds\n" \
"s_add_u32 m0, s57, 2880\n" \
"s_nop 0\n" \
"buffer_load_dwordx4 v1, s[68:71], s50 offen offset:192 lds\n" \
"s_add_u32 m0, s57, 3840\n" \
"s_nop 0\n" \
"buffer_load_dwordx4 v1, s[68:71], s50 offen offset:256 lds\n" \
"s_add_u32 m0, s57, 4800\n" \
"s_nop 0\n" \
"buffer_load_dwordx4 v1, s[68:71], s50 offen offset:320 lds\n" \
"s_add_u32 m0, s57, 5760\n" \
"s_nop 0\n" \
"buffer_load_dwordx4 v1, s[68:71], s50 offen offset:384 lds\n" \
"s_add_u32 m0, s57, 6720\n" \
"s_nop 0\n" \
"buffer_load_dwordx4 v1, s[68:71], s50 offen offset:448 lds\n" \
"s_add_u32 s50, s50, 65536\n" \
"s_min_u32 s50, s50, 2064384\n" \
"s_xor_b32 s57, s57, 32768\n" \
"v_bfe_u32 v127, v126, 2, 2\n" \
"v_mul_u32_u24 v112, 8192, v127\n" \
"v_bfe_u32 v127, v126, 4, 1\n" \
"v_mul_u32_u24 v127, 65568, v127\n" \
"v_add_u32 v112, v112, v127\n" \
"v_bfe_u32 v127, v126, 5, 1\n" \
"v_mul_u32_u24 v127, 131072, v127\n" \
"v_add_u32 v112, v112, v127\n" \
"v_bfe_u32 v127, v126, 0, 1\n" \
"v_mul_u32_u24 v127, 16, v127\n" \
"v_add_u32 v112, v112, v127\n" \
"v_and_b32 v127, 2, v126\n" \
"v_lshlrev_b32 v127, 4, v127\n" \
"v_xor_b32 v112, v112, v127\n" \
"v_add_u32 v115, 291840, v112\n" \
"v_add_u32 v114, 260096, v112\n" \
"v_add_u32 v113, 31744, v112\n" \
"s_mul_i32 s46, s36, 24576\n" \
"s_add_u32 s46, s46, 65536\n" \
"s_mov_b32 s58, s57\n" \
"s_lshl_b32 s59, s59, 21\n" \
"s_lshl_b32 s43, s37, 8\n" \
"s_lshl_b32 s44, s36, 6\n" \
"s_add_u32 s48, s43, s44\n" \
"s_mov_b32 s60, s86\n" \
"s_and_b32 s61, s87, 0xffff\n" \
"s_mov_b32 s62, 4194304\n" \
"s_mov_b32 s63, 0x20000\n" \
"s_xor_b32 s47, s59, 0\n" \
"s_add_u32 s47, s47, s48\n" \
"s_add_u32 m0, s46, 0\n" \
"s_nop 0\n" \
"buffer_load_dwordx4 v112, s[60:63], s47 offen offset:0 lds\n" \
"buffer_load_dwordx4 v113, s[60:63], s47 offen offset:1024 lds\n" \
"buffer_load_dwordx4 v114, s[60:63], s47 offen offset:2048 lds\n" \
"buffer_load_dwordx4 v115, s[60:63], s47 offen offset:3072 lds\n" \
"s_xor_b32 s47, s59, 524288\n" \
"s_add_u32 s47, s47, s48\n" \
"s_add_u32 m0, s46, 4096\n" \
"s_nop 0\n" \
"buffer_load_dwordx4 v112, s[60:63], s47 offen offset:0 lds\n" \
"buffer_load_dwordx4 v113, s[60:63], s47 offen offset:1024 lds\n" \
"buffer_load_dwordx4 v114, s[60:63], s47 offen offset:2048 lds\n" \
"buffer_load_dwordx4 v115, s[60:63], s47 offen offset:3072 lds\n" \
"s_xor_b32 s47, s59, 1048576\n" \
"s_add_u32 s47, s47, s48\n" \
"s_add_u32 m0, s46, 8192\n" \
"s_nop 0\n" \
"buffer_load_dwordx4 v112, s[60:63], s47 offen offset:0 lds\n" \
"buffer_load_dwordx4 v113, s[60:63], s47 offen offset:1024 lds\n" \
"buffer_load_dwordx4 v114, s[60:63], s47 offen offset:2048 lds\n" \
"buffer_load_dwordx4 v115, s[60:63], s47 offen offset:3072 lds\n" \
"s_xor_b32 s47, s59, 1572864\n" \
"s_add_u32 s47, s47, s48\n" \
"s_add_u32 m0, s46, 12288\n" \
"s_nop 0\n" \
"buffer_load_dwordx4 v112, s[60:63], s47 offen offset:0 lds\n" \
"buffer_load_dwordx4 v113, s[60:63], s47 offen offset:1024 lds\n" \
"buffer_load_dwordx4 v114, s[60:63], s47 offen offset:2048 lds\n" \
"buffer_load_dwordx4 v115, s[60:63], s47 offen offset:3072 lds\n" \
"s_xor_b32 s47, s59, 2097152\n" \
"s_add_u32 s47, s47, s48\n" \
"s_add_u32 m0, s46, 16384\n" \
"s_nop 0\n" \
"buffer_load_dwordx4 v112, s[60:63], s47 offen offset:0 lds\n" \
"buffer_load_dwordx4 v113, s[60:63], s47 offen offset:1024 lds\n" \
"buffer_load_dwordx4 v114, s[60:63], s47 offen offset:2048 lds\n" \
"buffer_load_dwordx4 v115, s[60:63], s47 offen offset:3072 lds\n" \
"s_xor_b32 s47, s59, 2621440\n" \
"s_add_u32 s47, s47, s48\n" \
"s_add_u32 m0, s46, 20480\n" \
"s_nop 0\n" \
"buffer_load_dwordx4 v112, s[60:63], s47 offen offset:0 lds\n" \
"buffer_load_dwordx4 v113, s[60:63], s47 offen offset:1024 lds\n" \
"buffer_load_dwordx4 v114, s[60:63], s47 offen offset:2048 lds\n" \
"buffer_load_dwordx4 v115, s[60:63], s47 offen offset:3072 lds\n" \
"s_xor_b32 s47, s59, 3145728\n" \
"s_add_u32 s47, s47, s48\n" \
"s_add_u32 m0, s58, 0\n" \
"s_nop 0\n" \
"buffer_load_dwordx4 v112, s[60:63], s47 offen offset:0 lds\n" \
"buffer_load_dwordx4 v113, s[60:63], s47 offen offset:1024 lds\n" \
"buffer_load_dwordx4 v114, s[60:63], s47 offen offset:2048 lds\n" \
"buffer_load_dwordx4 v115, s[60:63], s47 offen offset:3072 lds\n" \
"s_xor_b32 s47, s59, 3670016\n" \
"s_add_u32 s47, s47, s48\n" \
"s_add_u32 m0, s58, 4096\n" \
"s_nop 0\n" \
"buffer_load_dwordx4 v112, s[60:63], s47 offen offset:0 lds\n" \
"buffer_load_dwordx4 v113, s[60:63], s47 offen offset:1024 lds\n" \
"buffer_load_dwordx4 v114, s[60:63], s47 offen offset:2048 lds\n" \
"buffer_load_dwordx4 v115, s[60:63], s47 offen offset:3072 lds\n" \
"v_bfe_u32 v127, v126, 4, 2\n" \
"v_mul_u32_u24 v116, 256, v127\n" \
"v_bfe_u32 v127, v126, 2, 2\n" \
"v_mul_u32_u24 v127, 64, v127\n" \
"v_add_u32 v116, v116, v127\n" \
"v_bfe_u32 v127, v126, 4, 1\n" \
"v_mul_u32_u24 v127, 32, v127\n" \
"v_add_u32 v116, v116, v127\n" \
"v_bfe_u32 v127, v126, 0, 2\n" \
"v_mul_u32_u24 v127, 8, v127\n" \
"v_add_u32 v116, v116, v127\n" \
"v_add_u32 v116, s46, v116\n" \
"v_xor_b32 v117, 32, v116\n" \
"s_sub_u32 s43, s58, s46\n" \
"v_add_u32 v118, s43, v116\n" \
"v_add_u32 v119, s43, v117\n" \
"v_lshlrev_b32 v0, 4, v126\n" \
"v_bfe_u32 v127, v126, 1, 1\n" \
"v_mul_u32_u24 v2, 0xaaaa, v127\n" \
"v_add_u32 v2, 0x4444, v2\n" \
"s_lshl_b32 s43, s36, 11\n" \
"v_add_u32 v20, s43, v0\n" \
"v_add_u32 v22, 8192, v20\n" \
"s_lshr_b32 s44, s59, 9\n" \
"s_xor_b32 s43, s43, s44\n" \
"s_and_b32 s44, s43, 4096\n" \
"s_mul_i32 s44, s44, 3\n" \
"s_add_u32 s43, s43, s44\n" \
"v_bfe_u32 v127, v126, 0, 4\n" \
"v_mul_u32_u24 v3, 16, v127\n" \
"v_bfe_u32 v127, v126, 4, 2\n" \
"v_mul_u32_u24 v127, 4, v127\n" \
"v_add_u32 v3, v3, v127\n" \
"v_add_u32 v3, s43, v3\n" \
"s_lshl_b32 s43, s36, 10\n" \
"v_add_u32 v21, s43, v0\n" \
"s_add_u32 s43, s43, 65536\n" \
"v_bfe_u32 v127, v126, 0, 4\n" \
"v_mul_u32_u24 v4, 32, v127\n" \
"v_bfe_u32 v127, v126, 4, 2\n" \
"v_mul_u32_u24 v127, 8, v127\n" \
"v_add_u32 v4, v4, v127\n" \
"v_add_u32 v4, s43, v4\n" \
"v_lshlrev_b32 v5, 3, v126\n" \
"v_add_u32 v5, 65536, v5\n" \
"s_lshr_b32 s43, s36, 1\n" \
"s_lshl_b32 s43, s43, 11\n" \
"s_add_u32 s43, s43, 65540\n" \
"v_bfe_u32 v127, v126, 0, 4\n" \
"v_mul_u32_u24 v6, 32, v127\n" \
"v_bfe_u32 v127, v126, 4, 2\n" \
"v_mul_u32_u24 v127, 8, v127\n" \
"v_add_u32 v6, v6, v127\n" \
"v_add_u32 v6, s43, v6\n" \
"s_and_b32 s44, s36, 1\n" \
"s_lshl_b32 s43, s44, 17\n" \
"s_lshr_b32 s47, s36, 1\n" \
"s_lshl_b32 s47, s47, 10\n" \
"s_add_u32 s43, s43, s47\n" \
"v_bfe_u32 v127, v126, 3, 1\n" \
"v_mul_u32_u24 v7, 65536, v127\n" \
"v_bfe_u32 v127, v126, 4, 2\n" \
"v_mul_u32_u24 v127, 256, v127\n" \
"v_add_u32 v7, v7, v127\n" \
"v_bfe_u32 v127, v126, 0, 3\n" \
"v_mul_u32_u24 v127, 16, v127\n" \
"v_add_u32 v7, v7, v127\n" \
"v_add_u32 v7, s43, v7\n" \
"s_lshl_b32 s44, s44, 4\n" \
"v_and_b32 v124, 15, v126\n" \
"v_add_u32 v124, s44, v124\n" \
"v_lshrrev_b32 v125, 4, v126\n" \
"v_lshlrev_b32 v125, 2, v125\n" \
"v_sub_u32 v124, v124, v125\n" \
"v_mov_b32 v125, 0xff800000\n" \
"v_cmp_gt_i32 vcc, 0, v124\n" \
"s_nop 1\n" \
"v_cndmask_b32 v12, 0, v125, vcc\n" \
"v_cmp_gt_i32 vcc, 1, v124\n" \
"s_nop 1\n" \
"v_cndmask_b32 v13, 0, v125, vcc\n" \
"v_cmp_gt_i32 vcc, 2, v124\n" \
"s_nop 1\n" \
"v_cndmask_b32 v14, 0, v125, vcc\n" \
"v_cmp_gt_i32 vcc, 3, v124\n" \
"s_nop 1\n" \
"v_cndmask_b32 v15, 0, v125, vcc\n" \
"v_cmp_gt_i32 vcc, 16, v124\n" \
"s_nop 1\n" \
"v_cndmask_b32 v16, 0, v125, vcc\n" \
"v_cmp_gt_i32 vcc, 17, v124\n" \
"s_nop 1\n" \
"v_cndmask_b32 v17, 0, v125, vcc\n" \
"v_cmp_gt_i32 vcc, 18, v124\n" \
"s_nop 1\n" \
"v_cndmask_b32 v18, 0, v125, vcc\n" \
"v_cmp_gt_i32 vcc, 19, v124\n" \
"s_nop 1\n" \
"v_cndmask_b32 v19, 0, v125, vcc\n" \
"v_mov_b32 v8, 0x3f803f80\n" \
"v_mov_b32 v9, 0x3f803f80\n" \
"v_mov_b32 v10, 0x3f803f80\n" \
"v_mov_b32 v11, 0x3f803f80\n" \
"v_bfe_u32 v127, v126, 2, 2\n" \
"v_mul_u32_u24 v25, 2048, v127\n" \
"v_bfe_u32 v127, v126, 0, 1\n" \
"v_mul_u32_u24 v127, 1024, v127\n" \
"v_add_u32 v25, v25, v127\n" \
"v_bfe_u32 v127, v126, 4, 2\n" \
"v_mul_u32_u24 v127, 8, v127\n" \
"v_add_u32 v25, v25, v127\n" \
"v_bfe_u32 v127, v126, 1, 1\n" \
"v_mul_u32_u24 v127, 4, v127\n" \
"v_add_u32 v25, v25, v127\n" \
"s_lshl_b32 s43, s38, 19\n" \
"s_lshl_b32 s44, s37, 14\n" \
"s_add_u32 s51, s43, s44\n" \
"s_add_u32 s51, s51, 8388608\n" \
"s_lshl_b32 s43, s38, 7\n" \
"s_lshl_b32 s44, s37, 8\n" \
"s_add_u32 s52, s43, s44\n" \
"s_add_u32 s52, s52, 2621440\n" \
"s_add_u32 s53, s52, 8192\n" \
"s_lshl_b32 s43, s38, 18\n" \
"s_lshl_b32 s44, s37, 11\n" \
"s_add_u32 s54, s43, s44\n" \
"s_add_u32 s54, s54, 41943040\n" \
"s_mov_b32 s56, s38\n" \
"s_mov_b32 s40, 0\n" \
"s_mov_b32 s45, 1040356334\n" \
"s_mov_b32 s49, 0x40000\n" \
"s_mov_b32 s41, 65536\n" \
"s_mov_b32 s42, 65536\n" \
"s_waitcnt vmcnt(40)\n" \
"v_readfirstlane_b32 s97, v123\n" \
"s_add_u32 s97, s97, 256\n" \
"s_lshr_b32 s97, s97, 8\n" \
"v_mov_b32 v123, s97\n" \
"s_add_u32 s99, s96, 2686976\n" \
"s_mov_b64 exec, 1\n" \
"buffer_store_dword v123, off, s[64:67], s99 sc1\n" \
"s_mov_b64 exec, -1\n" \
"s_waitcnt vmcnt(29)\n" \
"ds_read_b64_tr_b16 a[0:1], v116 offset:0\n" \
"ds_read_b64_tr_b16 a[2:3], v116 offset:1024\n" \
"ds_read_b64_tr_b16 a[4:5], v116 offset:2048\n" \
"ds_read_b64_tr_b16 a[6:7], v116 offset:3072\n" \
"ds_read_b64_tr_b16 a[64:65], v117 offset:0\n" \
"ds_read_b64_tr_b16 a[66:67], v117 offset:1024\n" \
"ds_read_b64_tr_b16 a[68:69], v117 offset:2048\n" \
"ds_read_b64_tr_b16 a[70:71], v117 offset:3072\n" \
"s_waitcnt vmcnt(25)\n" \
"ds_read_b64_tr_b16 a[8:9], v116 offset:4096\n" \
"ds_read_b64_tr_b16 a[10:11], v116 offset:5120\n" \
"ds_read_b64_tr_b16 a[12:13], v116 offset:6144\n" \
"ds_read_b64_tr_b16 a[14:15], v116 offset:7168\n" \
"s_waitcnt lgkmcnt(4)\n" \
"ds_read_b64_tr_b16 a[72:73], v117 offset:4096\n" \
"ds_read_b64_tr_b16 a[74:75], v117 offset:5120\n" \
"ds_read_b64_tr_b16 a[76:77], v117 offset:6144\n" \
"ds_read_b64_tr_b16 a[78:79], v117 offset:7168\n" \
"s_mov_b32 s60, s88\n" \
"s_and_b32 s61, s89, 0xffff\n" \
"s_mov_b32 s62, 4194304\n" \
"s_mov_b32 s63, 0x20000\n" \
"s_xor_b32 s47, s59, 0\n" \
"s_add_u32 s47, s47, s48\n" \
"s_add_u32 m0, s46, 0\n" \
"s_nop 0\n" \
"buffer_load_dwordx4 v112, s[60:63], s47 offen offset:0 lds\n" \
"buffer_load_dwordx4 v113, s[60:63], s47 offen offset:1024 lds\n" \
"buffer_load_dwordx4 v114, s[60:63], s47 offen offset:2048 lds\n" \
"buffer_load_dwordx4 v115, s[60:63], s47 offen offset:3072 lds\n" \
"s_waitcnt vmcnt(25)\n" \
"ds_read_b64_tr_b16 a[16:17], v116 offset:8192\n" \
"ds_read_b64_tr_b16 a[18:19], v116 offset:9216\n" \
"ds_read_b64_tr_b16 a[20:21], v116 offset:10240\n" \
"ds_read_b64_tr_b16 a[22:23], v116 offset:11264\n" \
"s_waitcnt lgkmcnt(4)\n" \
"ds_read_b64_tr_b16 a[80:81], v117 offset:8192\n" \
"ds_read_b64_tr_b16 a[82:83], v117 offset:9216\n" \
"ds_read_b64_tr_b16 a[84:85], v117 offset:10240\n" \
"ds_read_b64_tr_b16 a[86:87], v117 offset:11264\n" \
"s_xor_b32 s47, s59, 524288\n" \
"s_add_u32 s47, s47, s48\n" \
"s_add_u32 m0, s46, 4096\n" \
"s_nop 0\n" \
"buffer_load_dwordx4 v112, s[60:63], s47 offen offset:0 lds\n" \
"buffer_load_dwordx4 v113, s[60:63], s47 offen offset:1024 lds\n" \
"buffer_load_dwordx4 v114, s[60:63], s47 offen offset:2048 lds\n" \
"buffer_load_dwordx4 v115, s[60:63], s47 offen offset:3072 lds\n" \
"s_waitcnt vmcnt(25)\n" \
"ds_read_b64_tr_b16 a[24:25], v116 offset:12288\n" \
"ds_read_b64_tr_b16 a[26:27], v116 offset:13312\n" \
"ds_read_b64_tr_b16 a[28:29], v116 offset:14336\n" \
"ds_read_b64_tr_b16 a[30:31], v116 offset:15360\n" \
"s_waitcnt lgkmcnt(4)\n" \
"ds_read_b64_tr_b16 a[88:89], v117 offset:12288\n" \
"ds_read_b64_tr_b16 a[90:91], v117 offset:13312\n" \
"ds_read_b64_tr_b16 a[92:93], v117 offset:14336\n" \
"ds_read_b64_tr_b16 a[94:95], v117 offset:15360\n" \
"s_xor_b32 s47, s59, 1048576\n" \
"s_add_u32 s47, s47, s48\n" \
"s_add_u32 m0, s46, 8192\n" \
"s_nop 0\n" \
"buffer_load_dwordx4 v112, s[60:63], s47 offen offset:0 lds\n" \
"buffer_load_dwordx4 v113, s[60:63], s47 offen offset:1024 lds\n" \
"buffer_load_dwordx4 v114, s[60:63], s47 offen offset:2048 lds\n" \
"buffer_load_dwordx4 v115, s[60:63], s47 offen offset:3072 lds\n" \
"s_waitcnt vmcnt(25)\n" \
"ds_read_b64_tr_b16 a[32:33], v116 offset:16384\n" \
"ds_read_b64_tr_b16 a[34:35], v116 offset:17408\n" \
"ds_read_b64_tr_b16 a[36:37], v116 offset:18432\n" \
"ds_read_b64_tr_b16 a[38:39], v116 offset:19456\n" \
"s_waitcnt lgkmcnt(4)\n" \
"ds_read_b64_tr_b16 a[96:97], v117 offset:16384\n" \
"ds_read_b64_tr_b16 a[98:99], v117 offset:17408\n" \
"ds_read_b64_tr_b16 a[100:101], v117 offset:18432\n" \
"ds_read_b64_tr_b16 a[102:103], v117 offset:19456\n" \
"s_xor_b32 s47, s59, 1572864\n" \
"s_add_u32 s47, s47, s48\n" \
"s_add_u32 m0, s46, 12288\n" \
"s_nop 0\n" \
"buffer_load_dwordx4 v112, s[60:63], s47 offen offset:0 lds\n" \
"buffer_load_dwordx4 v113, s[60:63], s47 offen offset:1024 lds\n" \
"buffer_load_dwordx4 v114, s[60:63], s47 offen offset:2048 lds\n" \
"buffer_load_dwordx4 v115, s[60:63], s47 offen offset:3072 lds\n" \
"s_waitcnt vmcnt(25)\n" \
"ds_read_b64_tr_b16 a[40:41], v116 offset:20480\n" \
"ds_read_b64_tr_b16 a[42:43], v116 offset:21504\n" \
"ds_read_b64_tr_b16 a[44:45], v116 offset:22528\n" \
"ds_read_b64_tr_b16 a[46:47], v116 offset:23552\n" \
"s_waitcnt lgkmcnt(4)\n" \
"ds_read_b64_tr_b16 a[104:105], v117 offset:20480\n" \
"ds_read_b64_tr_b16 a[106:107], v117 offset:21504\n" \
"ds_read_b64_tr_b16 a[108:109], v117 offset:22528\n" \
"ds_read_b64_tr_b16 a[110:111], v117 offset:23552\n" \
"s_xor_b32 s47, s59, 2097152\n" \
"s_add_u32 s47, s47, s48\n" \
"s_add_u32 m0, s46, 16384\n" \
"s_nop 0\n" \
"buffer_load_dwordx4 v112, s[60:63], s47 offen offset:0 lds\n" \
"buffer_load_dwordx4 v113, s[60:63], s47 offen offset:1024 lds\n" \
"buffer_load_dwordx4 v114, s[60:63], s47 offen offset:2048 lds\n" \
"buffer_load_dwordx4 v115, s[60:63], s47 offen offset:3072 lds\n" \
"s_waitcnt vmcnt(25)\n" \
"ds_read_b64_tr_b16 a[48:49], v118 offset:0\n" \
"ds_read_b64_tr_b16 a[50:51], v118 offset:1024\n" \
"ds_read_b64_tr_b16 a[52:53], v118 offset:2048\n" \
"ds_read_b64_tr_b16 a[54:55], v118 offset:3072\n" \
"s_waitcnt lgkmcnt(4)\n" \
"ds_read_b64_tr_b16 a[112:113], v119 offset:0\n" \
"ds_read_b64_tr_b16 a[114:115], v119 offset:1024\n" \
"ds_read_b64_tr_b16 a[116:117], v119 offset:2048\n" \
"ds_read_b64_tr_b16 a[118:119], v119 offset:3072\n" \
"s_xor_b32 s47, s59, 2621440\n" \
"s_add_u32 s47, s47, s48\n" \
"s_add_u32 m0, s46, 20480\n" \
"s_nop 0\n" \
"buffer_load_dwordx4 v112, s[60:63], s47 offen offset:0 lds\n" \
"buffer_load_dwordx4 v113, s[60:63], s47 offen offset:1024 lds\n" \
"buffer_load_dwordx4 v114, s[60:63], s47 offen offset:2048 lds\n" \
"buffer_load_dwordx4 v115, s[60:63], s47 offen offset:3072 lds\n" \
"s_waitcnt vmcnt(25)\n" \
"ds_read_b64_tr_b16 a[56:57], v118 offset:4096\n" \
"ds_read_b64_tr_b16 a[58:59], v118 offset:5120\n" \
"ds_read_b64_tr_b16 a[60:61], v118 offset:6144\n" \
"ds_read_b64_tr_b16 a[62:63], v118 offset:7168\n" \
"s_waitcnt lgkmcnt(4)\n" \
"ds_read_b64_tr_b16 a[120:121], v119 offset:4096\n" \
"ds_read_b64_tr_b16 a[122:123], v119 offset:5120\n" \
"ds_read_b64_tr_b16 a[124:125], v119 offset:6144\n" \
"ds_read_b64_tr_b16 a[126:127], v119 offset:7168\n" \
"s_xor_b32 s47, s59, 3145728\n" \
"s_add_u32 s47, s47, s48\n" \
"s_add_u32 m0, s58, 0\n" \
"s_nop 0\n" \
"buffer_load_dwordx4 v112, s[60:63], s47 offen offset:0 lds\n" \
"buffer_load_dwordx4 v113, s[60:63], s47 offen offset:1024 lds\n" \
"buffer_load_dwordx4 v114, s[60:63], s47 offen offset:2048 lds\n" \
"buffer_load_dwordx4 v115, s[60:63], s47 offen offset:3072 lds\n" \
"s_waitcnt vmcnt(24)\n" \
"ds_read_b64_tr_b16 a[128:129], v116 offset:0\n" \
"ds_read_b64_tr_b16 a[130:131], v116 offset:1024\n" \
"ds_read_b64_tr_b16 a[132:133], v116 offset:2048\n" \
"ds_read_b64_tr_b16 a[134:135], v116 offset:3072\n" \
"s_waitcnt lgkmcnt(4)\n" \
"ds_read_b64_tr_b16 a[192:193], v117 offset:0\n" \
"ds_read_b64_tr_b16 a[194:195], v117 offset:1024\n" \
"ds_read_b64_tr_b16 a[196:197], v117 offset:2048\n" \
"ds_read_b64_tr_b16 a[198:199], v117 offset:3072\n" \
"s_xor_b32 s47, s59, 3670016\n" \
"s_add_u32 s47, s47, s48\n" \
"s_add_u32 m0, s58, 4096\n" \
"s_nop 0\n" \
"buffer_load_dwordx4 v112, s[60:63], s47 offen offset:0 lds\n" \
"buffer_load_dwordx4 v113, s[60:63], s47 offen offset:1024 lds\n" \
"buffer_load_dwordx4 v114, s[60:63], s47 offen offset:2048 lds\n" \
"buffer_load_dwordx4 v115, s[60:63], s47 offen offset:3072 lds\n" \
"s_waitcnt vmcnt(24)\n" \
"ds_read_b64_tr_b16 a[136:137], v116 offset:4096\n" \
"ds_read_b64_tr_b16 a[138:139], v116 offset:5120\n" \
"ds_read_b64_tr_b16 a[140:141], v116 offset:6144\n" \
"ds_read_b64_tr_b16 a[142:143], v116 offset:7168\n" \
"s_waitcnt lgkmcnt(4)\n" \
"ds_read_b64_tr_b16 a[200:201], v117 offset:4096\n" \
"ds_read_b64_tr_b16 a[202:203], v117 offset:5120\n" \
"ds_read_b64_tr_b16 a[204:205], v117 offset:6144\n" \
"ds_read_b64_tr_b16 a[206:207], v117 offset:7168\n" \
"s_mov_b32 s60, s84\n" \
"s_and_b32 s61, s85, 0xffff\n" \
"s_mov_b32 s62, 4194304\n" \
"s_mov_b32 s63, 0x20000\n" \
"s_xor_b32 s47, s59, 0\n" \
"s_add_u32 s47, s47, s48\n" \
"s_add_u32 m0, s46, 0\n" \
"s_nop 0\n" \
"buffer_load_dwordx4 v112, s[60:63], s47 offen offset:0 lds\n" \
"buffer_load_dwordx4 v113, s[60:63], s47 offen offset:1024 lds\n" \
"buffer_load_dwordx4 v114, s[60:63], s47 offen offset:2048 lds\n" \
"buffer_load_dwordx4 v115, s[60:63], s47 offen offset:3072 lds\n" \
"s_waitcnt vmcnt(24)\n" \
"ds_read_b64_tr_b16 a[144:145], v116 offset:8192\n" \
"ds_read_b64_tr_b16 a[146:147], v116 offset:9216\n" \
"ds_read_b64_tr_b16 a[148:149], v116 offset:10240\n" \
"ds_read_b64_tr_b16 a[150:151], v116 offset:11264\n" \
"s_waitcnt lgkmcnt(4)\n" \
"ds_read_b64_tr_b16 a[208:209], v117 offset:8192\n" \
"ds_read_b64_tr_b16 a[210:211], v117 offset:9216\n" \
"ds_read_b64_tr_b16 a[212:213], v117 offset:10240\n" \
"ds_read_b64_tr_b16 a[214:215], v117 offset:11264\n" \
"s_xor_b32 s47, s59, 524288\n" \
"s_add_u32 s47, s47, s48\n" \
"s_add_u32 m0, s46, 4096\n" \
"s_nop 0\n" \
"buffer_load_dwordx4 v112, s[60:63], s47 offen offset:0 lds\n" \
"buffer_load_dwordx4 v113, s[60:63], s47 offen offset:1024 lds\n" \
"buffer_load_dwordx4 v114, s[60:63], s47 offen offset:2048 lds\n" \
"buffer_load_dwordx4 v115, s[60:63], s47 offen offset:3072 lds\n" \
"s_waitcnt vmcnt(24)\n" \
"ds_read_b64_tr_b16 a[152:153], v116 offset:12288\n" \
"ds_read_b64_tr_b16 a[154:155], v116 offset:13312\n" \
"ds_read_b64_tr_b16 a[156:157], v116 offset:14336\n" \
"ds_read_b64_tr_b16 a[158:159], v116 offset:15360\n" \
"s_waitcnt lgkmcnt(4)\n" \
"ds_read_b64_tr_b16 a[216:217], v117 offset:12288\n" \
"ds_read_b64_tr_b16 a[218:219], v117 offset:13312\n" \
"ds_read_b64_tr_b16 a[220:221], v117 offset:14336\n" \
"ds_read_b64_tr_b16 a[222:223], v117 offset:15360\n" \
"s_xor_b32 s47, s59, 1048576\n" \
"s_add_u32 s47, s47, s48\n" \
"s_add_u32 m0, s46, 8192\n" \
"s_nop 0\n" \
"buffer_load_dwordx4 v112, s[60:63], s47 offen offset:0 lds\n" \
"buffer_load_dwordx4 v113, s[60:63], s47 offen offset:1024 lds\n" \
"buffer_load_dwordx4 v114, s[60:63], s47 offen offset:2048 lds\n" \
"buffer_load_dwordx4 v115, s[60:63], s47 offen offset:3072 lds\n" \
"s_waitcnt vmcnt(24)\n" \
"ds_read_b64_tr_b16 a[160:161], v116 offset:16384\n" \
"ds_read_b64_tr_b16 a[162:163], v116 offset:17408\n" \
"ds_read_b64_tr_b16 a[164:165], v116 offset:18432\n" \
"ds_read_b64_tr_b16 a[166:167], v116 offset:19456\n" \
"s_waitcnt lgkmcnt(4)\n" \
"ds_read_b64_tr_b16 a[224:225], v117 offset:16384\n" \
"ds_read_b64_tr_b16 a[226:227], v117 offset:17408\n" \
"ds_read_b64_tr_b16 a[228:229], v117 offset:18432\n" \
"ds_read_b64_tr_b16 a[230:231], v117 offset:19456\n" \
"s_xor_b32 s47, s59, 1572864\n" \
"s_add_u32 s47, s47, s48\n" \
"s_add_u32 m0, s46, 12288\n" \
"s_nop 0\n" \
"buffer_load_dwordx4 v112, s[60:63], s47 offen offset:0 lds\n" \
"buffer_load_dwordx4 v113, s[60:63], s47 offen offset:1024 lds\n" \
"buffer_load_dwordx4 v114, s[60:63], s47 offen offset:2048 lds\n" \
"buffer_load_dwordx4 v115, s[60:63], s47 offen offset:3072 lds\n" \
"s_waitcnt vmcnt(24)\n" \
"ds_read_b64_tr_b16 a[168:169], v116 offset:20480\n" \
"ds_read_b64_tr_b16 a[170:171], v116 offset:21504\n" \
"ds_read_b64_tr_b16 a[172:173], v116 offset:22528\n" \
"ds_read_b64_tr_b16 a[174:175], v116 offset:23552\n" \
"s_waitcnt lgkmcnt(4)\n" \
"ds_read_b64_tr_b16 a[232:233], v117 offset:20480\n" \
"ds_read_b64_tr_b16 a[234:235], v117 offset:21504\n" \
"ds_read_b64_tr_b16 a[236:237], v117 offset:22528\n" \
"ds_read_b64_tr_b16 a[238:239], v117 offset:23552\n" \
"s_xor_b32 s47, s59, 2097152\n" \
"s_add_u32 s47, s47, s48\n" \
"s_add_u32 m0, s46, 16384\n" \
"s_nop 0\n" \
"buffer_load_dwordx4 v112, s[60:63], s47 offen offset:0 lds\n" \
"buffer_load_dwordx4 v113, s[60:63], s47 offen offset:1024 lds\n" \
"buffer_load_dwordx4 v114, s[60:63], s47 offen offset:2048 lds\n" \
"buffer_load_dwordx4 v115, s[60:63], s47 offen offset:3072 lds\n" \
"s_waitcnt vmcnt(24)\n" \
"ds_read_b64_tr_b16 a[176:177], v118 offset:0\n" \
"ds_read_b64_tr_b16 a[178:179], v118 offset:1024\n" \
"ds_read_b64_tr_b16 a[180:181], v118 offset:2048\n" \
"ds_read_b64_tr_b16 a[182:183], v118 offset:3072\n" \
"s_waitcnt lgkmcnt(4)\n" \
"ds_read_b64_tr_b16 a[240:241], v119 offset:0\n" \
"ds_read_b64_tr_b16 a[242:243], v119 offset:1024\n" \
"ds_read_b64_tr_b16 a[244:245], v119 offset:2048\n" \
"ds_read_b64_tr_b16 a[246:247], v119 offset:3072\n" \
"s_xor_b32 s47, s59, 2621440\n" \
"s_add_u32 s47, s47, s48\n" \
"s_add_u32 m0, s46, 20480\n" \
"s_nop 0\n" \
"buffer_load_dwordx4 v112, s[60:63], s47 offen offset:0 lds\n" \
"buffer_load_dwordx4 v113, s[60:63], s47 offen offset:1024 lds\n" \
"buffer_load_dwordx4 v114, s[60:63], s47 offen offset:2048 lds\n" \
"buffer_load_dwordx4 v115, s[60:63], s47 offen offset:3072 lds\n" \
"s_waitcnt vmcnt(24)\n" \
"ds_read_b64_tr_b16 a[184:185], v118 offset:4096\n" \
"ds_read_b64_tr_b16 a[186:187], v118 offset:5120\n" \
"ds_read_b64_tr_b16 a[188:189], v118 offset:6144\n" \
"ds_read_b64_tr_b16 a[190:191], v118 offset:7168\n" \
"s_waitcnt lgkmcnt(4)\n" \
"ds_read_b64_tr_b16 a[248:249], v119 offset:4096\n" \
"ds_read_b64_tr_b16 a[250:251], v119 offset:5120\n" \
"ds_read_b64_tr_b16 a[252:253], v119 offset:6144\n" \
"ds_read_b64_tr_b16 a[254:255], v119 offset:7168\n" \
"s_xor_b32 s47, s59, 3145728\n" \
"s_add_u32 s47, s47, s48\n" \
"s_add_u32 m0, s58, 0\n" \
"s_nop 0\n" \
"buffer_load_dwordx4 v112, s[60:63], s47 offen offset:0 lds\n" \
"buffer_load_dwordx4 v113, s[60:63], s47 offen offset:1024 lds\n" \
"buffer_load_dwordx4 v114, s[60:63], s47 offen offset:2048 lds\n" \
"buffer_load_dwordx4 v115, s[60:63], s47 offen offset:3072 lds\n" \
"s_waitcnt lgkmcnt(0)\n" \
"s_xor_b32 s47, s59, 3670016\n" \
"s_add_u32 s47, s47, s48\n" \
"s_add_u32 m0, s58, 4096\n" \
"s_nop 0\n" \
"buffer_load_dwordx4 v112, s[60:63], s47 offen offset:0 lds\n" \
"buffer_load_dwordx4 v113, s[60:63], s47 offen offset:1024 lds\n" \
"buffer_load_dwordx4 v114, s[60:63], s47 offen offset:2048 lds\n" \
"buffer_load_dwordx4 v115, s[60:63], s47 offen offset:3072 lds\n" \
"s_add_u32 s99, s96, 2719744\n" \
"buffer_load_dword v123, off, s[64:67], s99 sc1\n" \
"s_mov_b32 s60, s82\n" \
"s_and_b32 s61, s83, 0xffff\n" \
"s_mov_b32 s62, 8192\n" \
"s_mov_b32 s63, 0x20000\n" \
"v_bfe_u32 v125, v126, 0, 4\n" \
"v_mul_u32_u24 v127, 64, v125\n" \
"v_bfe_u32 v125, v126, 4, 2\n" \
"v_mul_u32_u24 v125, 16, v125\n" \
"v_add_u32 v127, v127, v125\n" \
"v_and_b32 v125, 4, v126\n" \
"v_lshlrev_b32 v125, 3, v125\n" \
"v_xor_b32 v127, v127, v125\n" \
"s_mov_b32 s46, 0x05040100\n" \
"s_mov_b32 s48, 0x07060302\n" \
"s_mov_b32 s58, 0\n" \
"v_mov_b32 v125, 0\n" \
"s_lshl_b32 s43, s36, 2\n" \
"s_add_u32 s53, s53, s43\n" \
"s_barrier\n" \
"ds_read_b128 v[28:31], v127 offset:0\n" \
"ds_read_b128 v[32:35], v127 offset:16384\n" \
"ds_read_b128 v[36:39], v127 offset:1024\n" \
"ds_read_b128 v[40:43], v127 offset:17408\n" \
"v_add_u32 v23, s41, v21\n" \
"v_add_u32 v24, s41, v22\n" \
"s_waitcnt lgkmcnt(3)\n" \
"v_mfma_f32_16x16x32_bf16 v[52:55], a[0:3], v[28:31], 0\n" \
"s_waitcnt lgkmcnt(2)\n" \
"v_mfma_f32_16x16x32_bf16 v[60:63], a[0:3], v[32:35], 0\n" \
"ds_read_b128 v[44:47], v127 offset:2048\n" \
"v_mfma_f32_16x16x32_bf16 v[56:59], a[64:67], v[28:31], 0\n" \
"v_mfma_f32_16x16x32_bf16 v[64:67], a[64:67], v[32:35], 0\n" \
"ds_read_b128 v[48:51], v127 offset:18432\n" \
"v_mfma_f32_16x16x32_bf16 v[68:71], v[28:31], a[128:131], 0\n" \
"v_mfma_f32_16x16x32_bf16 v[72:75], v[32:35], a[128:131], 0\n" \
"v_mfma_f32_16x16x32_bf16 v[76:79], v[28:31], a[192:195], 0\n" \
"v_mfma_f32_16x16x32_bf16 v[80:83], v[32:35], a[192:195], 0\n" \
"s_waitcnt lgkmcnt(3)\n" \
"v_mfma_f32_16x16x32_bf16 v[52:55], a[4:7], v[36:39], v[52:55]\n" \
"s_waitcnt lgkmcnt(2)\n" \
"v_mfma_f32_16x16x32_bf16 v[60:63], a[4:7], v[40:43], v[60:63]\n" \
"ds_read_b128 v[28:31], v127 offset:3072\n" \
"v_mfma_f32_16x16x32_bf16 v[56:59], a[68:71], v[36:39], v[56:59]\n" \
"v_mfma_f32_16x16x32_bf16 v[64:67], a[68:71], v[40:43], v[64:67]\n" \
"ds_read_b128 v[32:35], v127 offset:19456\n" \
"v_mfma_f32_16x16x32_bf16 v[68:71], v[36:39], a[132:135], v[68:71]\n" \
"v_mfma_f32_16x16x32_bf16 v[72:75], v[40:43], a[132:135], v[72:75]\n" \
"v_mfma_f32_16x16x32_bf16 v[76:79], v[36:39], a[196:199], v[76:79]\n" \
"v_mfma_f32_16x16x32_bf16 v[80:83], v[40:43], a[196:199], v[80:83]\n" \
"s_waitcnt lgkmcnt(3)\n" \
"v_mfma_f32_16x16x32_bf16 v[52:55], a[8:11], v[44:47], v[52:55]\n" \
"s_waitcnt lgkmcnt(2)\n" \
"v_mfma_f32_16x16x32_bf16 v[60:63], a[8:11], v[48:51], v[60:63]\n" \
"ds_read_b128 v[36:39], v127 offset:4096\n" \
"v_mfma_f32_16x16x32_bf16 v[56:59], a[72:75], v[44:47], v[56:59]\n" \
"v_mfma_f32_16x16x32_bf16 v[64:67], a[72:75], v[48:51], v[64:67]\n" \
"ds_read_b128 v[40:43], v127 offset:20480\n" \
"v_mfma_f32_16x16x32_bf16 v[68:71], v[44:47], a[136:139], v[68:71]\n" \
"v_mfma_f32_16x16x32_bf16 v[72:75], v[48:51], a[136:139], v[72:75]\n" \
"v_mfma_f32_16x16x32_bf16 v[76:79], v[44:47], a[200:203], v[76:79]\n" \
"v_mfma_f32_16x16x32_bf16 v[80:83], v[48:51], a[200:203], v[80:83]\n" \
"s_waitcnt lgkmcnt(3)\n" \
"v_mfma_f32_16x16x32_bf16 v[52:55], a[12:15], v[28:31], v[52:55]\n" \
"s_waitcnt lgkmcnt(2)\n" \
"v_mfma_f32_16x16x32_bf16 v[60:63], a[12:15], v[32:35], v[60:63]\n" \
"ds_read_b128 v[44:47], v127 offset:5120\n" \
"v_mfma_f32_16x16x32_bf16 v[56:59], a[76:79], v[28:31], v[56:59]\n" \
"v_mfma_f32_16x16x32_bf16 v[64:67], a[76:79], v[32:35], v[64:67]\n" \
"ds_read_b128 v[48:51], v127 offset:21504\n" \
"v_mfma_f32_16x16x32_bf16 v[68:71], v[28:31], a[140:143], v[68:71]\n" \
"v_mfma_f32_16x16x32_bf16 v[72:75], v[32:35], a[140:143], v[72:75]\n" \
"v_mfma_f32_16x16x32_bf16 v[76:79], v[28:31], a[204:207], v[76:79]\n" \
"v_mfma_f32_16x16x32_bf16 v[80:83], v[32:35], a[204:207], v[80:83]\n" \
"s_waitcnt lgkmcnt(3)\n" \
"v_mfma_f32_16x16x32_bf16 v[52:55], a[16:19], v[36:39], v[52:55]\n" \
"s_waitcnt lgkmcnt(2)\n" \
"v_mfma_f32_16x16x32_bf16 v[60:63], a[16:19], v[40:43], v[60:63]\n" \
"ds_read_b128 v[28:31], v127 offset:6144\n" \
"v_mfma_f32_16x16x32_bf16 v[56:59], a[80:83], v[36:39], v[56:59]\n" \
"v_mfma_f32_16x16x32_bf16 v[64:67], a[80:83], v[40:43], v[64:67]\n" \
"ds_read_b128 v[32:35], v127 offset:22528\n" \
"v_mfma_f32_16x16x32_bf16 v[68:71], v[36:39], a[144:147], v[68:71]\n" \
"v_mfma_f32_16x16x32_bf16 v[72:75], v[40:43], a[144:147], v[72:75]\n" \
"v_mfma_f32_16x16x32_bf16 v[76:79], v[36:39], a[208:211], v[76:79]\n" \
"v_mfma_f32_16x16x32_bf16 v[80:83], v[40:43], a[208:211], v[80:83]\n" \
"s_waitcnt lgkmcnt(3)\n" \
"v_mfma_f32_16x16x32_bf16 v[52:55], a[20:23], v[44:47], v[52:55]\n" \
"s_waitcnt lgkmcnt(2)\n" \
"v_mfma_f32_16x16x32_bf16 v[60:63], a[20:23], v[48:51], v[60:63]\n" \
"ds_read_b128 v[36:39], v127 offset:7168\n" \
"v_mfma_f32_16x16x32_bf16 v[56:59], a[84:87], v[44:47], v[56:59]\n" \
"v_mfma_f32_16x16x32_bf16 v[64:67], a[84:87], v[48:51], v[64:67]\n" \
"ds_read_b128 v[40:43], v127 offset:23552\n" \
"v_mfma_f32_16x16x32_bf16 v[68:71], v[44:47], a[148:151], v[68:71]\n" \
"v_mfma_f32_16x16x32_bf16 v[72:75], v[48:51], a[148:151], v[72:75]\n" \
"v_mfma_f32_16x16x32_bf16 v[76:79], v[44:47], a[212:215], v[76:79]\n" \
"v_mfma_f32_16x16x32_bf16 v[80:83], v[48:51], a[212:215], v[80:83]\n" \
"s_waitcnt lgkmcnt(3)\n" \
"v_mfma_f32_16x16x32_bf16 v[52:55], a[24:27], v[28:31], v[52:55]\n" \
"s_waitcnt lgkmcnt(2)\n" \
"v_mfma_f32_16x16x32_bf16 v[60:63], a[24:27], v[32:35], v[60:63]\n" \
"ds_read_b128 v[44:47], v127 offset:8192\n" \
"v_mfma_f32_16x16x32_bf16 v[56:59], a[88:91], v[28:31], v[56:59]\n" \
"v_mfma_f32_16x16x32_bf16 v[64:67], a[88:91], v[32:35], v[64:67]\n" \
"ds_read_b128 v[48:51], v127 offset:24576\n" \
"v_mfma_f32_16x16x32_bf16 v[68:71], v[28:31], a[152:155], v[68:71]\n" \
"v_mfma_f32_16x16x32_bf16 v[72:75], v[32:35], a[152:155], v[72:75]\n" \
"v_mfma_f32_16x16x32_bf16 v[76:79], v[28:31], a[216:219], v[76:79]\n" \
"v_mfma_f32_16x16x32_bf16 v[80:83], v[32:35], a[216:219], v[80:83]\n" \
"s_waitcnt lgkmcnt(3)\n" \
"v_mfma_f32_16x16x32_bf16 v[52:55], a[28:31], v[36:39], v[52:55]\n" \
"s_waitcnt lgkmcnt(2)\n" \
"v_mfma_f32_16x16x32_bf16 v[60:63], a[28:31], v[40:43], v[60:63]\n" \
"ds_read_b128 v[28:31], v127 offset:9216\n" \
"v_mfma_f32_16x16x32_bf16 v[56:59], a[92:95], v[36:39], v[56:59]\n" \
"v_mfma_f32_16x16x32_bf16 v[64:67], a[92:95], v[40:43], v[64:67]\n" \
"ds_read_b128 v[32:35], v127 offset:25600\n" \
"v_mfma_f32_16x16x32_bf16 v[68:71], v[36:39], a[156:159], v[68:71]\n" \
"v_mfma_f32_16x16x32_bf16 v[72:75], v[40:43], a[156:159], v[72:75]\n" \
"v_mfma_f32_16x16x32_bf16 v[76:79], v[36:39], a[220:223], v[76:79]\n" \
"v_mfma_f32_16x16x32_bf16 v[80:83], v[40:43], a[220:223], v[80:83]\n" \
"s_waitcnt lgkmcnt(3)\n" \
"v_mfma_f32_16x16x32_bf16 v[52:55], a[32:35], v[44:47], v[52:55]\n" \
"s_waitcnt lgkmcnt(2)\n" \
"v_mfma_f32_16x16x32_bf16 v[60:63], a[32:35], v[48:51], v[60:63]\n" \
"ds_read_b128 v[36:39], v127 offset:10240\n" \
"v_mfma_f32_16x16x32_bf16 v[56:59], a[96:99], v[44:47], v[56:59]\n" \
"v_mfma_f32_16x16x32_bf16 v[64:67], a[96:99], v[48:51], v[64:67]\n" \
"ds_read_b128 v[40:43], v127 offset:26624\n" \
"v_mfma_f32_16x16x32_bf16 v[68:71], v[44:47], a[160:163], v[68:71]\n" \
"v_mfma_f32_16x16x32_bf16 v[72:75], v[48:51], a[160:163], v[72:75]\n" \
"v_mfma_f32_16x16x32_bf16 v[76:79], v[44:47], a[224:227], v[76:79]\n" \
"v_mfma_f32_16x16x32_bf16 v[80:83], v[48:51], a[224:227], v[80:83]\n" \
"s_waitcnt lgkmcnt(3)\n" \
"v_mfma_f32_16x16x32_bf16 v[52:55], a[36:39], v[28:31], v[52:55]\n" \
"s_waitcnt lgkmcnt(2)\n" \
"v_mfma_f32_16x16x32_bf16 v[60:63], a[36:39], v[32:35], v[60:63]\n" \
"ds_read_b128 v[44:47], v127 offset:11264\n" \
"v_mfma_f32_16x16x32_bf16 v[56:59], a[100:103], v[28:31], v[56:59]\n" \
"v_mfma_f32_16x16x32_bf16 v[64:67], a[100:103], v[32:35], v[64:67]\n" \
"ds_read_b128 v[48:51], v127 offset:27648\n" \
"v_mfma_f32_16x16x32_bf16 v[68:71], v[28:31], a[164:167], v[68:71]\n" \
"v_mfma_f32_16x16x32_bf16 v[72:75], v[32:35], a[164:167], v[72:75]\n" \
"v_mfma_f32_16x16x32_bf16 v[76:79], v[28:31], a[228:231], v[76:79]\n" \
"v_mfma_f32_16x16x32_bf16 v[80:83], v[32:35], a[228:231], v[80:83]\n" \
"s_waitcnt lgkmcnt(3)\n" \
"v_mfma_f32_16x16x32_bf16 v[52:55], a[40:43], v[36:39], v[52:55]\n" \
"s_waitcnt lgkmcnt(2)\n" \
"v_mfma_f32_16x16x32_bf16 v[60:63], a[40:43], v[40:43], v[60:63]\n" \
"ds_read_b128 v[28:31], v127 offset:12288\n" \
"v_mfma_f32_16x16x32_bf16 v[56:59], a[104:107], v[36:39], v[56:59]\n" \
"v_mfma_f32_16x16x32_bf16 v[64:67], a[104:107], v[40:43], v[64:67]\n" \
"ds_read_b128 v[32:35], v127 offset:28672\n" \
"v_mfma_f32_16x16x32_bf16 v[68:71], v[36:39], a[168:171], v[68:71]\n" \
"v_mfma_f32_16x16x32_bf16 v[72:75], v[40:43], a[168:171], v[72:75]\n" \
"v_mfma_f32_16x16x32_bf16 v[76:79], v[36:39], a[232:235], v[76:79]\n" \
"v_mfma_f32_16x16x32_bf16 v[80:83], v[40:43], a[232:235], v[80:83]\n" \
"s_waitcnt lgkmcnt(3)\n" \
"v_mfma_f32_16x16x32_bf16 v[52:55], a[44:47], v[44:47], v[52:55]\n" \
"s_waitcnt lgkmcnt(2)\n" \
"v_mfma_f32_16x16x32_bf16 v[60:63], a[44:47], v[48:51], v[60:63]\n" \
"ds_read_b128 v[36:39], v127 offset:13312\n" \
"v_mfma_f32_16x16x32_bf16 v[56:59], a[108:111], v[44:47], v[56:59]\n" \
"v_mfma_f32_16x16x32_bf16 v[64:67], a[108:111], v[48:51], v[64:67]\n" \
"ds_read_b128 v[40:43], v127 offset:29696\n" \
"v_mfma_f32_16x16x32_bf16 v[68:71], v[44:47], a[172:175], v[68:71]\n" \
"v_mfma_f32_16x16x32_bf16 v[72:75], v[48:51], a[172:175], v[72:75]\n" \
"v_mfma_f32_16x16x32_bf16 v[76:79], v[44:47], a[236:239], v[76:79]\n" \
"v_mfma_f32_16x16x32_bf16 v[80:83], v[48:51], a[236:239], v[80:83]\n" \
"s_waitcnt lgkmcnt(3)\n" \
"v_mfma_f32_16x16x32_bf16 v[52:55], a[48:51], v[28:31], v[52:55]\n" \
"s_waitcnt lgkmcnt(2)\n" \
"v_mfma_f32_16x16x32_bf16 v[60:63], a[48:51], v[32:35], v[60:63]\n" \
"ds_read_b128 v[44:47], v127 offset:14336\n" \
"v_mfma_f32_16x16x32_bf16 v[56:59], a[112:115], v[28:31], v[56:59]\n" \
"v_mfma_f32_16x16x32_bf16 v[64:67], a[112:115], v[32:35], v[64:67]\n" \
"ds_read_b128 v[48:51], v127 offset:30720\n" \
"v_mfma_f32_16x16x32_bf16 v[68:71], v[28:31], a[176:179], v[68:71]\n" \
"v_mfma_f32_16x16x32_bf16 v[72:75], v[32:35], a[176:179], v[72:75]\n" \
"v_mfma_f32_16x16x32_bf16 v[76:79], v[28:31], a[240:243], v[76:79]\n" \
"v_mfma_f32_16x16x32_bf16 v[80:83], v[32:35], a[240:243], v[80:83]\n" \
"s_waitcnt lgkmcnt(3)\n" \
"v_mfma_f32_16x16x32_bf16 v[52:55], a[52:55], v[36:39], v[52:55]\n" \
"s_waitcnt lgkmcnt(2)\n" \
"v_mfma_f32_16x16x32_bf16 v[60:63], a[52:55], v[40:43], v[60:63]\n" \
"ds_read_b128 v[28:31], v127 offset:15360\n" \
"v_mfma_f32_16x16x32_bf16 v[56:59], a[116:119], v[36:39], v[56:59]\n" \
"v_mfma_f32_16x16x32_bf16 v[64:67], a[116:119], v[40:43], v[64:67]\n" \
"ds_read_b128 v[32:35], v127 offset:31744\n" \
"v_mfma_f32_16x16x32_bf16 v[68:71], v[36:39], a[180:183], v[68:71]\n" \
"v_mfma_f32_16x16x32_bf16 v[72:75], v[40:43], a[180:183], v[72:75]\n" \
"v_mfma_f32_16x16x32_bf16 v[76:79], v[36:39], a[244:247], v[76:79]\n" \
"v_mfma_f32_16x16x32_bf16 v[80:83], v[40:43], a[244:247], v[80:83]\n" \
"s_waitcnt lgkmcnt(3)\n" \
"v_mfma_f32_16x16x32_bf16 v[52:55], a[56:59], v[44:47], v[52:55]\n" \
"s_waitcnt lgkmcnt(2)\n" \
"v_mfma_f32_16x16x32_bf16 v[60:63], a[56:59], v[48:51], v[60:63]\n" \
"ds_read_b128 v[36:39], v127 offset:0\n" \
"v_mfma_f32_16x16x32_bf16 v[56:59], a[120:123], v[44:47], v[56:59]\n" \
"v_mfma_f32_16x16x32_bf16 v[64:67], a[120:123], v[48:51], v[64:67]\n" \
"ds_read_b128 v[40:43], v127 offset:16384\n" \
"v_mfma_f32_16x16x32_bf16 v[68:71], v[44:47], a[184:187], v[68:71]\n" \
"v_mfma_f32_16x16x32_bf16 v[72:75], v[48:51], a[184:187], v[72:75]\n" \
"v_mfma_f32_16x16x32_bf16 v[76:79], v[44:47], a[248:251], v[76:79]\n" \
"v_mfma_f32_16x16x32_bf16 v[80:83], v[48:51], a[248:251], v[80:83]\n" \
"s_waitcnt lgkmcnt(3)\n" \
"v_mfma_f32_16x16x32_bf16 v[52:55], a[60:63], v[28:31], v[52:55]\n" \
"s_waitcnt lgkmcnt(2)\n" \
"v_mfma_f32_16x16x32_bf16 v[60:63], a[60:63], v[32:35], v[60:63]\n" \
"ds_read_b128 v[44:47], v127 offset:1024\n" \
"v_mfma_f32_16x16x32_bf16 v[56:59], a[124:127], v[28:31], v[56:59]\n" \
"v_mfma_f32_16x16x32_bf16 v[64:67], a[124:127], v[32:35], v[64:67]\n" \
"ds_read_b128 v[48:51], v127 offset:17408\n" \
"v_mfma_f32_16x16x32_bf16 v[68:71], v[28:31], a[188:191], v[68:71]\n" \
"v_mfma_f32_16x16x32_bf16 v[72:75], v[32:35], a[188:191], v[72:75]\n" \
"v_mul_f32 v52, s45, v52\n" \
"v_mul_f32 v53, s45, v53\n" \
"v_mfma_f32_16x16x32_bf16 v[76:79], v[28:31], a[252:255], v[76:79]\n" \
"v_mul_f32 v54, s45, v54\n" \
"v_mul_f32 v55, s45, v55\n" \
"v_mfma_f32_16x16x32_bf16 v[80:83], v[32:35], a[252:255], v[80:83]\n" \
"v_mul_f32 v56, s45, v56\n" \
"v_mul_f32 v57, s45, v57\n" \
"v_mul_f32 v58, s45, v58\n" \
"v_mul_f32 v59, s45, v59\n" \
"v_cvt_pk_bf16_f32 v52, v52, v53\n" \
"v_cvt_pk_bf16_f32 v53, v54, v55\n" \
"v_cvt_pk_bf16_f32 v54, v56, v57\n" \
"v_cvt_pk_bf16_f32 v55, v58, v59\n" \
"buffer_store_dwordx4 v[52:55], v21, s[64:67], s51 offen offset:0 sc1\n" \
"v_mul_f32 v60, s45, v60\n" \
"v_mul_f32 v61, s45, v61\n" \
"v_mul_f32 v62, s45, v62\n" \
"v_mul_f32 v63, s45, v63\n" \
"v_mul_f32 v64, s45, v64\n" \
"v_mul_f32 v65, s45, v65\n" \
"v_mul_f32 v66, s45, v66\n" \
"v_mul_f32 v67, s45, v67\n" \
"v_cvt_pk_bf16_f32 v60, v60, v61\n" \
"v_cvt_pk_bf16_f32 v61, v62, v63\n" \
"v_cvt_pk_bf16_f32 v62, v64, v65\n" \
"v_cvt_pk_bf16_f32 v63, v66, v67\n" \
"s_add_u32 s43, s51, 4096\n" \
"buffer_store_dwordx4 v[60:63], v21, s[64:67], s43 offen sc1\n" \
"v_cvt_pk_bf16_f32 v68, v68, v69\n" \
"v_cvt_pk_bf16_f32 v69, v70, v71\n" \
"v_cvt_pk_bf16_f32 v70, v72, v73\n" \
"v_cvt_pk_bf16_f32 v71, v74, v75\n" \
"buffer_store_dwordx4 v[68:71], v22, s[64:67], s51 offen offset:0 sc1\n" \
"v_cvt_pk_bf16_f32 v76, v76, v77\n" \
"v_cvt_pk_bf16_f32 v77, v78, v79\n" \
"v_cvt_pk_bf16_f32 v78, v80, v81\n" \
"v_cvt_pk_bf16_f32 v79, v82, v83\n" \
"buffer_store_dwordx4 v[76:79], v22, s[64:67], s51 offen offset:1024 sc1\n" \
"s_add_u32 s43, s38, 1\n" \
"v_mov_b32 v124, s43\n" \
"s_waitcnt vmcnt(4)\n" \
"s_bfe_u32 s98, %[id], 458760\n" \
"s_bitcmp1_b32 %[id], 15\n" \
"s_cbranch_scc0 .Lx_hn_%=\n" \
"s_waitcnt vmcnt(0)\n" \
"s_branch .Lx_hk_%=\n" \
".Lx_hn_%=:\n" \
"s_mov_b32 s47, 0x40000\n" \
".Lx_hc_%=:\n" \
"v_readfirstlane_b32 s98, v123\n" \
"s_lshr_b32 s99, s98, 8\n" \
"s_cmp_eq_u32 s99, s97\n" \
"s_cbranch_scc1 .Lx_hg_%=\n" \
"s_sleep 2\n" \
"s_add_u32 s99, s96, 2719744\n" \
"buffer_load_dword v123, off, s[64:67], s99 sc1\n" \
"s_waitcnt vmcnt(0)\n" \
"s_sub_u32 s47, s47, 1\n" \
"s_cbranch_scc0 .Lx_hc_%=\n" \
".Lx_hg_%=:\n" \
"v_mov_b32 v123, 0\n" \
"s_add_u32 s99, s96, 2641920\n" \
"s_mov_b64 exec, 1\n" \
"buffer_store_dword v123, off, s[64:67], s99 sc1\n" \
"s_mov_b64 exec, -1\n" \
"s_lshr_b32 s99, %[id], 8\n" \
"s_xor_b32 s99, s99, s98\n" \
"s_and_b32 s99, s99, 0x70\n" \
"s_cbranch_scc1 .Lx_redo_%=\n" \
".Lx_hk_%=:\n" \
"s_lshr_b32 s43, s37, 2\n" \
"s_xor_b32 s43, s43, s98\n" \
"s_and_b32 s43, s43, 2\n" \
"s_andn2_b32 s39, 1, s98\n" \
"s_or_b32 s39, s39, s43\n" \
"s_lshl_b32 s39, s39, 13\n" \
"s_and_b32 s43, s98, 4\n" \
"s_lshl_b32 s43, s43, 11\n" \
"s_xor_b32 s52, s52, s43\n" \
"s_xor_b32 s53, s53, s43\n" \
"ds_read_b64_tr_b16 v[176:177], v118 offset:0\n" \
"ds_read_b64_tr_b16 v[178:179], v118 offset:1024\n" \
"ds_read_b64_tr_b16 v[180:181], v118 offset:2048\n" \
"ds_read_b64_tr_b16 v[182:183], v118 offset:3072\n" \
"s_waitcnt lgkmcnt(4)\n" \
"ds_read_b64_tr_b16 v[240:241], v119 offset:0\n" \
"ds_read_b64_tr_b16 v[242:243], v119 offset:1024\n" \
"ds_read_b64_tr_b16 v[244:245], v119 offset:2048\n" \
"ds_read_b64_tr_b16 v[246:247], v119 offset:3072\n" \
"ds_read_b64_tr_b16 v[184:185], v118 offset:4096\n" \
"ds_read_b64_tr_b16 v[186:187], v118 offset:5120\n" \
"ds_read_b64_tr_b16 v[188:189], v118 offset:6144\n" \
"ds_read_b64_tr_b16 v[190:191], v118 offset:7168\n" \
"s_waitcnt lgkmcnt(4)\n" \
"ds_read_b64_tr_b16 v[248:249], v119 offset:4096\n" \
"ds_read_b64_tr_b16 v[250:251], v119 offset:5120\n" \
"ds_read_b64_tr_b16 v[252:253], v119 offset:6144\n" \
"ds_read_b64_tr_b16 v[254:255], v119 offset:7168\n" \
"s_waitcnt vmcnt(1)\n" \
"s_barrier\n" \
"s_cmp_lg_u32 s36, 0\n" \
"s_cbranch_scc1 .Lx_f1_%=\n" \
"s_mov_b64 exec, 1\n" \
"buffer_store_dword v124, off, s[64:67], s52 sc1\n" \
"s_mov_b64 exec, -1\n" \
".Lx_f1_%=:\n" \
"s_waitcnt lgkmcnt(0)\n" \
"s_add_u32 m0, s57, 0\n" \
"s_nop 0\n" \
"buffer_load_dwordx4 v1, s[68:71], s50 offen offset:0 lds\n" \
"s_add_u32 m0, s57, 960\n" \
"s_nop 0\n" \
"buffer_load_dwordx4 v1, s[68:71], s50 offen offset:64 lds\n" \
"s_add_u32 m0, s57, 1920\n" \
"s_nop 0\n" \
"buffer_load_dwordx4 v1, s[68:71], s50 offen offset:128 lds\n" \
"s_add_u32 m0, s57, 2880\n" \
"s_nop 0\n" \
"buffer_load_dwordx4 v1, s[68:71], s50 offen offset:192 lds\n" \
"s_add_u32 m0, s57, 3840\n" \
"s_nop 0\n" \
"buffer_load_dwordx4 v1, s[68:71], s50 offen offset:256 lds\n" \
"s_add_u32 m0, s57, 4800\n" \
"s_nop 0\n" \
"buffer_load_dwordx4 v1, s[68:71], s50 offen offset:320 lds\n" \
"s_add_u32 m0, s57, 5760\n" \
"s_nop 0\n" \
"buffer_load_dwordx4 v1, s[68:71], s50 offen offset:384 lds\n" \
"s_add_u32 m0, s57, 6720\n" \
"s_nop 0\n" \
"buffer_load_dwordx4 v1, s[68:71], s50 offen offset:448 lds\n" \
"s_add_u32 s50, s50, 65536\n" \
"s_min_u32 s50, s50, 2064384\n" \
"s_xor_b32 s57, s57, 32768\n" \
"ds_read_b64_tr_b16 v[128:129], v116 offset:0\n" \
"ds_read_b64_tr_b16 v[130:131], v116 offset:1024\n" \
"ds_read_b64_tr_b16 v[132:133], v116 offset:2048\n" \
"ds_read_b64_tr_b16 v[134:135], v116 offset:3072\n" \
"ds_read_b64_tr_b16 v[192:193], v117 offset:0\n" \
"ds_read_b64_tr_b16 v[194:195], v117 offset:1024\n" \
"ds_read_b64_tr_b16 v[196:197], v117 offset:2048\n" \
"ds_read_b64_tr_b16 v[198:199], v117 offset:3072\n" \
"s_waitcnt lgkmcnt(4)\n" \
"ds_read_b64_tr_b16 v[136:137], v116 offset:4096\n" \
"ds_read_b64_tr_b16 v[138:139], v116 offset:5120\n" \
"ds_read_b64_tr_b16 v[140:141], v116 offset:6144\n" \
"ds_read_b64_tr_b16 v[142:143], v116 offset:7168\n" \
"ds_read_b64_tr_b16 v[200:201], v117 offset:4096\n" \
"ds_read_b64_tr_b16 v[202:203], v117 offset:5120\n" \
"ds_read_b64_tr_b16 v[204:205], v117 offset:6144\n" \
"ds_read_b64_tr_b16 v[206:207], v117 offset:7168\n" \
"s_waitcnt lgkmcnt(4)\n" \
"ds_read_b64_tr_b16 v[144:145], v116 offset:8192\n" \
"ds_read_b64_tr_b16 v[146:147], v116 offset:9216\n" \
"ds_read_b64_tr_b16 v[148:149], v116 offset:10240\n" \
"ds_read_b64_tr_b16 v[150:151], v116 offset:11264\n" \
"ds_read_b64_tr_b16 v[208:209], v117 offset:8192\n" \
"ds_read_b64_tr_b16 v[210:211], v117 offset:9216\n" \
"ds_read_b64_tr_b16 v[212:213], v117 offset:10240\n" \
"ds_read_b64_tr_b16 v[214:215], v117 offset:11264\n" \
"s_waitcnt lgkmcnt(4)\n" \
"ds_read_b64_tr_b16 v[152:153], v116 offset:12288\n" \
"ds_read_b64_tr_b16 v[154:155], v116 offset:13312\n" \
"ds_read_b64_tr_b16 v[156:157], v116 offset:14336\n" \
"ds_read_b64_tr_b16 v[158:159], v116 offset:15360\n" \
"ds_read_b64_tr_b16 v[216:217], v117 offset:12288\n" \
"ds_read_b64_tr_b16 v[218:219], v117 offset:13312\n" \
"ds_read_b64_tr_b16 v[220:221], v117 offset:14336\n" \
"ds_read_b64_tr_b16 v[222:223], v117 offset:15360\n" \
"s_waitcnt lgkmcnt(4)\n" \
"ds_read_b64_tr_b16 v[160:161], v116 offset:16384\n" \
"ds_read_b64_tr_b16 v[162:163], v116 offset:17408\n" \
"ds_read_b64_tr_b16 v[164:165], v116 offset:18432\n" \
"ds_read_b64_tr_b16 v[166:167], v116 offset:19456\n" \
"ds_read_b64_tr_b16 v[224:225], v117 offset:16384\n" \
"ds_read_b64_tr_b16 v[226:227], v117 offset:17408\n" \
"ds_read_b64_tr_b16 v[228:229], v117 offset:18432\n" \
"ds_read_b64_tr_b16 v[230:231], v117 offset:19456\n" \
"s_waitcnt lgkmcnt(4)\n" \
"ds_read_b64_tr_b16 v[168:169], v116 offset:20480\n" \
"ds_read_b64_tr_b16 v[170:171], v116 offset:21504\n" \
"ds_read_b64_tr_b16 v[172:173], v116 offset:22528\n" \
"ds_read_b64_tr_b16 v[174:175], v116 offset:23552\n" \
"ds_read_b64_tr_b16 v[232:233], v117 offset:20480\n" \
"ds_read_b64_tr_b16 v[234:235], v117 offset:21504\n" \
"ds_read_b64_tr_b16 v[236:237], v117 offset:22528\n" \
"ds_read_b64_tr_b16 v[238:239], v117 offset:23552\n" \
"s_waitcnt lgkmcnt(4)\n" \
"s_waitcnt lgkmcnt(0)\n" \
"s_barrier\n" \
"v_mfma_f32_16x16x32_bf16 v[84:87], v[128:131], v[36:39], 0\n" \
"v_mfma_f32_16x16x32_bf16 v[92:95], v[128:131], v[40:43], 0\n" \
"ds_read_b128 v[28:31], v127 offset:2048\n" \
"v_mfma_f32_16x16x32_bf16 v[88:91], v[192:195], v[36:39], 0\n" \
"v_mfma_f32_16x16x32_bf16 v[96:99], v[192:195], v[40:43], 0\n" \
"ds_read_b128 v[32:35], v127 offset:18432\n" \
"v_mfma_f32_16x16x32_bf16 v[84:87], v[132:135], v[44:47], v[84:87]\n" \
"v_mfma_f32_16x16x32_bf16 v[92:95], v[132:135], v[48:51], v[92:95]\n" \
"ds_read_b128 v[36:39], v127 offset:3072\n" \
"v_mfma_f32_16x16x32_bf16 v[88:91], v[196:199], v[44:47], v[88:91]\n" \
"v_mfma_f32_16x16x32_bf16 v[96:99], v[196:199], v[48:51], v[96:99]\n" \
"ds_read_b128 v[40:43], v127 offset:19456\n" \
"s_waitcnt lgkmcnt(3)\n" \
"v_mfma_f32_16x16x32_bf16 v[84:87], v[136:139], v[28:31], v[84:87]\n" \
"s_waitcnt lgkmcnt(2)\n" \
"v_mfma_f32_16x16x32_bf16 v[92:95], v[136:139], v[32:35], v[92:95]\n" \
"ds_read_b128 v[44:47], v127 offset:4096\n" \
"v_mfma_f32_16x16x32_bf16 v[88:91], v[200:203], v[28:31], v[88:91]\n" \
"v_mfma_f32_16x16x32_bf16 v[96:99], v[200:203], v[32:35], v[96:99]\n" \
"ds_read_b128 v[48:51], v127 offset:20480\n" \
"s_waitcnt lgkmcnt(3)\n" \
"v_mfma_f32_16x16x32_bf16 v[84:87], v[140:143], v[36:39], v[84:87]\n" \
"s_waitcnt lgkmcnt(2)\n" \
"v_mfma_f32_16x16x32_bf16 v[92:95], v[140:143], v[40:43], v[92:95]\n" \
"ds_read_b128 v[28:31], v127 offset:5120\n" \
"v_mfma_f32_16x16x32_bf16 v[88:91], v[204:207], v[36:39], v[88:91]\n" \
"v_mfma_f32_16x16x32_bf16 v[96:99], v[204:207], v[40:43], v[96:99]\n" \
"ds_read_b128 v[32:35], v127 offset:21504\n" \
"s_waitcnt lgkmcnt(3)\n" \
"v_mfma_f32_16x16x32_bf16 v[84:87], v[144:147], v[44:47], v[84:87]\n" \
"s_waitcnt lgkmcnt(2)\n" \
"v_mfma_f32_16x16x32_bf16 v[92:95], v[144:147], v[48:51], v[92:95]\n" \
"ds_read_b128 v[36:39], v127 offset:6144\n" \
"v_mfma_f32_16x16x32_bf16 v[88:91], v[208:211], v[44:47], v[88:91]\n" \
"v_mfma_f32_16x16x32_bf16 v[96:99], v[208:211], v[48:51], v[96:99]\n" \
"ds_read_b128 v[40:43], v127 offset:22528\n" \
"s_waitcnt lgkmcnt(3)\n" \
"v_mfma_f32_16x16x32_bf16 v[84:87], v[148:151], v[28:31], v[84:87]\n" \
"s_waitcnt lgkmcnt(2)\n" \
"v_mfma_f32_16x16x32_bf16 v[92:95], v[148:151], v[32:35], v[92:95]\n" \
"ds_read_b128 v[44:47], v127 offset:7168\n" \
"v_mfma_f32_16x16x32_bf16 v[88:91], v[212:215], v[28:31], v[88:91]\n" \
"v_mfma_f32_16x16x32_bf16 v[96:99], v[212:215], v[32:35], v[96:99]\n" \
"ds_read_b128 v[48:51], v127 offset:23552\n" \
"s_waitcnt lgkmcnt(3)\n" \
"v_mfma_f32_16x16x32_bf16 v[84:87], v[152:155], v[36:39], v[84:87]\n" \
"s_waitcnt lgkmcnt(2)\n" \
"v_mfma_f32_16x16x32_bf16 v[92:95], v[152:155], v[40:43], v[92:95]\n" \
"ds_read_b128 v[28:31], v127 offset:8192\n" \
"v_mfma_f32_16x16x32_bf16 v[88:91], v[216:219], v[36:39], v[88:91]\n" \
"v_mfma_f32_16x16x32_bf16 v[96:99], v[216:219], v[40:43], v[96:99]\n" \
"ds_read_b128 v[32:35], v127 offset:24576\n" \
"s_waitcnt lgkmcnt(3)\n" \
"v_mfma_f32_16x16x32_bf16 v[84:87], v[156:159], v[44:47], v[84:87]\n" \
"s_waitcnt lgkmcnt(2)\n" \
"v_mfma_f32_16x16x32_bf16 v[92:95], v[156:159], v[48:51], v[92:95]\n" \
"ds_read_b128 v[36:39], v127 offset:9216\n" \
"v_mfma_f32_16x16x32_bf16 v[88:91], v[220:223], v[44:47], v[88:91]\n" \
"v_mfma_f32_16x16x32_bf16 v[96:99], v[220:223], v[48:51], v[96:99]\n" \
"ds_read_b128 v[40:43], v127 offset:25600\n" \
"s_waitcnt lgkmcnt(3)\n" \
"v_mfma_f32_16x16x32_bf16 v[84:87], v[160:163], v[28:31], v[84:87]\n" \
"s_waitcnt lgkmcnt(2)\n" \
"v_mfma_f32_16x16x32_bf16 v[92:95], v[160:163], v[32:35], v[92:95]\n" \
"ds_read_b128 v[44:47], v127 offset:10240\n" \
"v_mfma_f32_16x16x32_bf16 v[88:91], v[224:227], v[28:31], v[88:91]\n" \
"v_mfma_f32_16x16x32_bf16 v[96:99], v[224:227], v[32:35], v[96:99]\n" \
"ds_read_b128 v[48:51], v127 offset:26624\n" \
"s_waitcnt lgkmcnt(3)\n" \
"v_mfma_f32_16x16x32_bf16 v[84:87], v[164:167], v[36:39], v[84:87]\n" \
"s_waitcnt lgkmcnt(2)\n" \
"v_mfma_f32_16x16x32_bf16 v[92:95], v[164:167], v[40:43], v[92:95]\n" \
"ds_read_b128 v[28:31], v127 offset:11264\n" \
"v_mfma_f32_16x16x32_bf16 v[88:91], v[228:231], v[36:39], v[88:91]\n" \
"v_mfma_f32_16x16x32_bf16 v[96:99], v[228:231], v[40:43], v[96:99]\n" \
"ds_read_b128 v[32:35], v127 offset:27648\n" \
"s_waitcnt lgkmcnt(3)\n" \
"v_mfma_f32_16x16x32_bf16 v[84:87], v[168:171], v[44:47], v[84:87]\n" \
"s_waitcnt lgkmcnt(2)\n" \
"v_mfma_f32_16x16x32_bf16 v[92:95], v[168:171], v[48:51], v[92:95]\n" \
"ds_read_b128 v[36:39], v127 offset:12288\n" \
"v_mfma_f32_16x16x32_bf16 v[88:91], v[232:235], v[44:47], v[88:91]\n" \
"v_mfma_f32_16x16x32_bf16 v[96:99], v[232:235], v[48:51], v[96:99]\n" \
"ds_read_b128 v[40:43], v127 offset:28672\n" \
"s_waitcnt lgkmcnt(3)\n" \
"v_mfma_f32_16x16x32_bf16 v[84:87], v[172:175], v[28:31], v[84:87]\n" \
"s_waitcnt lgkmcnt(2)\n" \
"v_mfma_f32_16x16x32_bf16 v[92:95], v[172:175], v[32:35], v[92:95]\n" \
"ds_read_b128 v[44:47], v127 offset:13312\n" \
"v_mfma_f32_16x16x32_bf16 v[88:91], v[236:239], v[28:31], v[88:91]\n" \
"v_mfma_f32_16x16x32_bf16 v[96:99], v[236:239], v[32:35], v[96:99]\n" \
"ds_read_b128 v[48:51], v127 offset:29696\n" \
"s_waitcnt lgkmcnt(3)\n" \
"v_mfma_f32_16x16x32_bf16 v[84:87], v[176:179], v[36:39], v[84:87]\n" \
"s_waitcnt lgkmcnt(2)\n" \
"v_mfma_f32_16x16x32_bf16 v[92:95], v[176:179], v[40:43], v[92:95]\n" \
"ds_read_b128 v[28:31], v127 offset:14336\n" \
"v_mfma_f32_16x16x32_bf16 v[88:91], v[240:243], v[36:39], v[88:91]\n" \
"v_mfma_f32_16x16x32_bf16 v[96:99], v[240:243], v[40:43], v[96:99]\n" \
"ds_read_b128 v[32:35], v127 offset:30720\n" \
"s_waitcnt lgkmcnt(3)\n" \
"v_mfma_f32_16x16x32_bf16 v[84:87], v[180:183], v[44:47], v[84:87]\n" \
"s_waitcnt lgkmcnt(2)\n" \
"v_mfma_f32_16x16x32_bf16 v[92:95], v[180:183], v[48:51], v[92:95]\n" \
"ds_read_b128 v[36:39], v127 offset:15360\n" \
"v_mfma_f32_16x16x32_bf16 v[88:91], v[244:247], v[44:47], v[88:91]\n" \
"v_mfma_f32_16x16x32_bf16 v[96:99], v[244:247], v[48:51], v[96:99]\n" \
"ds_read_b128 v[40:43], v127 offset:31744\n" \
"s_waitcnt lgkmcnt(3)\n" \
"v_mfma_f32_16x16x32_bf16 v[84:87], v[184:187], v[28:31], v[84:87]\n" \
"ds_write_b128 v23, v[52:55] offset:0\n" \
"s_waitcnt lgkmcnt(3)\n" \
"v_mfma_f32_16x16x32_bf16 v[92:95], v[184:187], v[32:35], v[92:95]\n" \
"ds_write_b128 v23, v[60:63] offset:4096\n" \
"v_mfma_f32_16x16x32_bf16 v[88:91], v[248:251], v[28:31], v[88:91]\n" \
"ds_write_b128 v24, v[68:71] offset:0\n" \
"v_mfma_f32_16x16x32_bf16 v[96:99], v[248:251], v[32:35], v[96:99]\n" \
"ds_write_b128 v24, v[76:79] offset:1024\n" \
"s_waitcnt lgkmcnt(5)\n" \
"v_mfma_f32_16x16x32_bf16 v[84:87], v[188:191], v[36:39], v[84:87]\n" \
"s_waitcnt lgkmcnt(4)\n" \
"v_mfma_f32_16x16x32_bf16 v[92:95], v[188:191], v[40:43], v[92:95]\n" \
"v_mfma_f32_16x16x32_bf16 v[88:91], v[252:255], v[36:39], v[88:91]\n" \
"v_mfma_f32_16x16x32_bf16 v[96:99], v[252:255], v[40:43], v[96:99]\n" \
"s_add_u32 s41, s41, 24576\n" \
"s_cmp_eq_u32 s41, 163840\n" \
"s_cselect_b32 s41, 65536, s41\n" \
"s_add_u32 s51, s51, 1048576\n" \
"v_xor_b32 v127, 32768, v127\n" \
"s_nop 1\n" \
"v_cvt_pk_bf16_f32 v84, v84, v85\n" \
"v_cvt_pk_bf16_f32 v85, v86, v87\n" \
"v_cvt_pk_bf16_f32 v86, v88, v89\n" \
"v_cvt_pk_bf16_f32 v87, v90, v91\n" \
"ds_write_b128 v23, v[84:87] offset:16384\n" \
"v_cvt_pk_bf16_f32 v92, v92, v93\n" \
"v_cvt_pk_bf16_f32 v93, v94, v95\n" \
"v_cvt_pk_bf16_f32 v94, v96, v97\n" \
"v_cvt_pk_bf16_f32 v95, v98, v99\n" \
"ds_write_b128 v23, v[92:95] offset:20480\n" \
"s_waitcnt vmcnt(0) lgkmcnt(0)\n" \
"s_barrier\n" \
"ds_read_b128 v[28:31], v127 offset:0\n" \
"ds_read_b128 v[32:35], v127 offset:16384\n" \
"ds_read_b128 v[36:39], v127 offset:1024\n" \
"ds_read_b128 v[40:43], v127 offset:17408\n" \
"s_branch .Lx_next_%=\n" \
".Lx_top_%=:\n" \
"s_xor_b32 s58, s58, 32768\n" \
"v_xor_b32 v3, 32768, v3\n" \
"v_add_u32 v23, s41, v21\n" \
"v_add_u32 v24, s41, v22\n" \
"s_waitcnt lgkmcnt(3)\n" \
"v_mfma_f32_16x16x32_bf16 v[52:55], a[0:3], v[28:31], 0\n" \
"s_waitcnt lgkmcnt(2)\n" \
"v_mfma_f32_16x16x32_bf16 v[60:63], a[0:3], v[32:35], 0\n" \
"ds_read_b128 v[44:47], v127 offset:2048\n" \
"v_mfma_f32_16x16x32_bf16 v[56:59], a[64:67], v[28:31], 0\n" \
"v_mfma_f32_16x16x32_bf16 v[64:67], a[64:67], v[32:35], 0\n" \
"ds_read_b128 v[48:51], v127 offset:18432\n" \
"v_mfma_f32_16x16x32_bf16 v[68:71], v[28:31], a[128:131], 0\n" \
"s_add_u32 m0, s57, 0\n" \
"s_nop 0\n" \
"buffer_load_dwordx4 v1, s[68:71], s50 offen offset:0 lds\n" \
"v_mfma_f32_16x16x32_bf16 v[72:75], v[32:35], a[128:131], 0\n" \
"s_waitcnt lgkmcnt(3)\n" \
"v_mfma_f32_16x16x32_bf16 v[52:55], a[4:7], v[36:39], v[52:55]\n" \
"s_waitcnt lgkmcnt(2)\n" \
"v_mfma_f32_16x16x32_bf16 v[60:63], a[4:7], v[40:43], v[60:63]\n" \
"ds_read_b128 v[28:31], v127 offset:3072\n" \
"v_mfma_f32_16x16x32_bf16 v[56:59], a[68:71], v[36:39], v[56:59]\n" \
"v_mfma_f32_16x16x32_bf16 v[64:67], a[68:71], v[40:43], v[64:67]\n" \
"ds_read_b128 v[32:35], v127 offset:19456\n" \
"v_mfma_f32_16x16x32_bf16 v[68:71], v[36:39], a[132:135], v[68:71]\n" \
"s_add_u32 m0, s57, 960\n" \
"s_nop 0\n" \
"buffer_load_dwordx4 v1, s[68:71], s50 offen offset:64 lds\n" \
"v_mfma_f32_16x16x32_bf16 v[72:75], v[40:43], a[132:135], v[72:75]\n" \
"s_waitcnt lgkmcnt(3)\n" \
"v_mfma_f32_16x16x32_bf16 v[52:55], a[8:11], v[44:47], v[52:55]\n" \
"s_waitcnt lgkmcnt(2)\n" \
"v_mfma_f32_16x16x32_bf16 v[60:63], a[8:11], v[48:51], v[60:63]\n" \
"ds_read_b128 v[36:39], v127 offset:4096\n" \
"v_mfma_f32_16x16x32_bf16 v[56:59], a[72:75], v[44:47], v[56:59]\n" \
"v_mfma_f32_16x16x32_bf16 v[64:67], a[72:75], v[48:51], v[64:67]\n" \
"ds_read_b128 v[40:43], v127 offset:20480\n" \
"v_mfma_f32_16x16x32_bf16 v[68:71], v[44:47], a[136:139], v[68:71]\n" \
"s_add_u32 m0, s57, 1920\n" \
"s_nop 0\n" \
"buffer_load_dwordx4 v1, s[68:71], s50 offen offset:128 lds\n" \
"v_mfma_f32_16x16x32_bf16 v[72:75], v[48:51], a[136:139], v[72:75]\n" \
"s_waitcnt lgkmcnt(3)\n" \
"v_mfma_f32_16x16x32_bf16 v[52:55], a[12:15], v[28:31], v[52:55]\n" \
"s_waitcnt lgkmcnt(2)\n" \
"v_mfma_f32_16x16x32_bf16 v[60:63], a[12:15], v[32:35], v[60:63]\n" \
"ds_read_b128 v[44:47], v127 offset:5120\n" \
"v_mfma_f32_16x16x32_bf16 v[56:59], a[76:79], v[28:31], v[56:59]\n" \
"v_mfma_f32_16x16x32_bf16 v[64:67], a[76:79], v[32:35], v[64:67]\n" \
"ds_read_b128 v[48:51], v127 offset:21504\n" \
"v_mfma_f32_16x16x32_bf16 v[68:71], v[28:31], a[140:143], v[68:71]\n" \
"s_add_u32 m0, s57, 2880\n" \
"s_nop 0\n" \
"buffer_load_dwordx4 v1, s[68:71], s50 offen offset:192 lds\n" \
"v_mfma_f32_16x16x32_bf16 v[72:75], v[32:35], a[140:143], v[72:75]\n" \
"s_waitcnt lgkmcnt(3)\n" \
"v_mfma_f32_16x16x32_bf16 v[52:55], a[16:19], v[36:39], v[52:55]\n" \
"s_waitcnt lgkmcnt(2)\n" \
"v_mfma_f32_16x16x32_bf16 v[60:63], a[16:19], v[40:43], v[60:63]\n" \
"ds_read_b128 v[28:31], v127 offset:6144\n" \
"v_mfma_f32_16x16x32_bf16 v[56:59], a[80:83], v[36:39], v[56:59]\n" \
"v_mfma_f32_16x16x32_bf16 v[64:67], a[80:83], v[40:43], v[64:67]\n" \
"ds_read_b128 v[32:35], v127 offset:22528\n" \
"v_mfma_f32_16x16x32_bf16 v[68:71], v[36:39], a[144:147], v[68:71]\n" \
"s_add_u32 m0, s57, 3840\n" \
"s_nop 0\n" \
"buffer_load_dwordx4 v1, s[68:71], s50 offen offset:256 lds\n" \
"v_mfma_f32_16x16x32_bf16 v[72:75], v[40:43], a[144:147], v[72:75]\n" \
"s_waitcnt lgkmcnt(3)\n" \
"v_mfma_f32_16x16x32_bf16 v[52:55], a[20:23], v[44:47], v[52:55]\n" \
"s_waitcnt lgkmcnt(2)\n" \
"v_mfma_f32_16x16x32_bf16 v[60:63], a[20:23], v[48:51], v[60:63]\n" \
"ds_read_b128 v[36:39], v127 offset:7168\n" \
"v_mfma_f32_16x16x32_bf16 v[56:59], a[84:87], v[44:47], v[56:59]\n" \
"v_mfma_f32_16x16x32_bf16 v[64:67], a[84:87], v[48:51], v[64:67]\n" \
"ds_read_b128 v[40:43], v127 offset:23552\n" \
"v_mfma_f32_16x16x32_bf16 v[68:71], v[44:47], a[148:151], v[68:71]\n" \
"s_add_u32 m0, s57, 4800\n" \
"s_nop 0\n" \
"buffer_load_dwordx4 v1, s[68:71], s50 offen offset:320 lds\n" \
"v_mfma_f32_16x16x32_bf16 v[72:75], v[48:51], a[148:151], v[72:75]\n" \
"s_waitcnt lgkmcnt(3)\n" \
"v_mfma_f32_16x16x32_bf16 v[52:55], a[24:27], v[28:31], v[52:55]\n" \
"s_waitcnt lgkmcnt(2)\n" \
"v_mfma_f32_16x16x32_bf16 v[60:63], a[24:27], v[32:35], v[60:63]\n" \
"ds_read_b128 v[44:47], v127 offset:8192\n" \
"v_mfma_f32_16x16x32_bf16 v[56:59], a[88:91], v[28:31], v[56:59]\n" \
"v_mfma_f32_16x16x32_bf16 v[64:67], a[88:91], v[32:35], v[64:67]\n" \
"ds_read_b128 v[48:51], v127 offset:24576\n" \
"v_mfma_f32_16x16x32_bf16 v[68:71], v[28:31], a[152:155], v[68:71]\n" \
"s_add_u32 m0, s57, 5760\n" \
"s_nop 0\n" \
"buffer_load_dwordx4 v1, s[68:71], s50 offen offset:384 lds\n" \
"v_mfma_f32_16x16x32_bf16 v[72:75], v[32:35], a[152:155], v[72:75]\n" \
"s_waitcnt lgkmcnt(3)\n" \
"v_mfma_f32_16x16x32_bf16 v[52:55], a[28:31], v[36:39], v[52:55]\n" \
"s_waitcnt lgkmcnt(2)\n" \
"v_mfma_f32_16x16x32_bf16 v[60:63], a[28:31], v[40:43], v[60:63]\n" \
"ds_read_b128 v[28:31], v127 offset:9216\n" \
"v_mfma_f32_16x16x32_bf16 v[56:59], a[92:95], v[36:39], v[56:59]\n" \
"v_mfma_f32_16x16x32_bf16 v[64:67], a[92:95], v[40:43], v[64:67]\n" \
"ds_read_b128 v[32:35], v127 offset:25600\n" \
"v_mfma_f32_16x16x32_bf16 v[68:71], v[36:39], a[156:159], v[68:71]\n" \
"s_add_u32 m0, s57, 6720\n" \
"s_nop 0\n" \
"buffer_load_dwordx4 v1, s[68:71], s50 offen offset:448 lds\n" \
"s_add_u32 s50, s50, 65536\n" \
"s_min_u32 s50, s50, 2064384\n" \
"s_xor_b32 s57, s57, 32768\n" \
"v_mfma_f32_16x16x32_bf16 v[72:75], v[40:43], a[156:159], v[72:75]\n" \
"s_waitcnt lgkmcnt(3)\n" \
"v_mfma_f32_16x16x32_bf16 v[52:55], a[32:35], v[44:47], v[52:55]\n" \
"s_waitcnt lgkmcnt(2)\n" \
"v_mfma_f32_16x16x32_bf16 v[60:63], a[32:35], v[48:51], v[60:63]\n" \
"ds_read_b128 v[36:39], v127 offset:10240\n" \
"v_mfma_f32_16x16x32_bf16 v[56:59], a[96:99], v[44:47], v[56:59]\n" \
"v_mfma_f32_16x16x32_bf16 v[64:67], a[96:99], v[48:51], v[64:67]\n" \
"ds_read_b128 v[40:43], v127 offset:26624\n" \
"v_mfma_f32_16x16x32_bf16 v[68:71], v[44:47], a[160:163], v[68:71]\n" \
"v_mfma_f32_16x16x32_bf16 v[72:75], v[48:51], a[160:163], v[72:75]\n" \
"s_waitcnt lgkmcnt(3)\n" \
"v_mfma_f32_16x16x32_bf16 v[52:55], a[36:39], v[28:31], v[52:55]\n" \
"s_waitcnt lgkmcnt(2)\n" \
"v_mfma_f32_16x16x32_bf16 v[60:63], a[36:39], v[32:35], v[60:63]\n" \
"ds_read_b128 v[44:47], v127 offset:11264\n" \
"v_mfma_f32_16x16x32_bf16 v[56:59], a[100:103], v[28:31], v[56:59]\n" \
"v_mfma_f32_16x16x32_bf16 v[64:67], a[100:103], v[32:35], v[64:67]\n" \
"ds_read_b128 v[48:51], v127 offset:27648\n" \
"v_mfma_f32_16x16x32_bf16 v[68:71], v[28:31], a[164:167], v[68:71]\n" \
"v_mfma_f32_16x16x32_bf16 v[72:75], v[32:35], a[164:167], v[72:75]\n" \
"s_waitcnt lgkmcnt(3)\n" \
"v_mfma_f32_16x16x32_bf16 v[52:55], a[40:43], v[36:39], v[52:55]\n" \
"s_waitcnt lgkmcnt(2)\n" \
"v_mfma_f32_16x16x32_bf16 v[60:63], a[40:43], v[40:43], v[60:63]\n" \
"ds_read_b128 v[28:31], v127 offset:12288\n" \
"v_mfma_f32_16x16x32_bf16 v[56:59], a[104:107], v[36:39], v[56:59]\n" \
"v_mfma_f32_16x16x32_bf16 v[64:67], a[104:107], v[40:43], v[64:67]\n" \
"ds_read_b128 v[32:35], v127 offset:28672\n" \
"v_mfma_f32_16x16x32_bf16 v[68:71], v[36:39], a[168:171], v[68:71]\n" \
"s_waitcnt vmcnt(8)\n" \
"s_mov_b64 exec, 1\n" \
"buffer_store_dword v125, off, s[64:67], s53 sc1\n" \
"s_mov_b64 exec, -1\n" \
"v_mfma_f32_16x16x32_bf16 v[72:75], v[40:43], a[168:171], v[72:75]\n" \
"s_waitcnt lgkmcnt(3)\n" \
"v_mfma_f32_16x16x32_bf16 v[52:55], a[44:47], v[44:47], v[52:55]\n" \
"s_waitcnt lgkmcnt(2)\n" \
"v_mfma_f32_16x16x32_bf16 v[60:63], a[44:47], v[48:51], v[60:63]\n" \
"ds_read_b128 v[36:39], v127 offset:13312\n" \
"v_mfma_f32_16x16x32_bf16 v[56:59], a[108:111], v[44:47], v[56:59]\n" \
"v_mfma_f32_16x16x32_bf16 v[64:67], a[108:111], v[48:51], v[64:67]\n" \
"ds_read_b128 v[40:43], v127 offset:29696\n" \
"v_mfma_f32_16x16x32_bf16 v[68:71], v[44:47], a[172:175], v[68:71]\n" \
"v_mfma_f32_16x16x32_bf16 v[72:75], v[48:51], a[172:175], v[72:75]\n" \
"s_waitcnt lgkmcnt(3)\n" \
"v_mfma_f32_16x16x32_bf16 v[52:55], a[48:51], v[28:31], v[52:55]\n" \
"s_waitcnt lgkmcnt(2)\n" \
"v_mfma_f32_16x16x32_bf16 v[60:63], a[48:51], v[32:35], v[60:63]\n" \
"ds_read_b128 v[44:47], v127 offset:14336\n" \
"v_mfma_f32_16x16x32_bf16 v[56:59], a[112:115], v[28:31], v[56:59]\n" \
"v_mfma_f32_16x16x32_bf16 v[64:67], a[112:115], v[32:35], v[64:67]\n" \
"ds_read_b128 v[48:51], v127 offset:30720\n" \
"v_mfma_f32_16x16x32_bf16 v[68:71], v[28:31], a[176:179], v[68:71]\n" \
"v_mfma_f32_16x16x32_bf16 v[72:75], v[32:35], a[176:179], v[72:75]\n" \
"s_waitcnt lgkmcnt(3)\n" \
"v_mfma_f32_16x16x32_bf16 v[52:55], a[52:55], v[36:39], v[52:55]\n" \
"s_waitcnt lgkmcnt(2)\n" \
"v_mfma_f32_16x16x32_bf16 v[60:63], a[52:55], v[40:43], v[60:63]\n" \
"ds_read_b128 v[28:31], v127 offset:15360\n" \
"v_mfma_f32_16x16x32_bf16 v[56:59], a[116:119], v[36:39], v[56:59]\n" \
"v_mfma_f32_16x16x32_bf16 v[64:67], a[116:119], v[40:43], v[64:67]\n" \
"ds_read_b128 v[32:35], v127 offset:31744\n" \
"v_mfma_f32_16x16x32_bf16 v[68:71], v[36:39], a[180:183], v[68:71]\n" \
"v_mfma_f32_16x16x32_bf16 v[72:75], v[40:43], a[180:183], v[72:75]\n" \
"s_waitcnt lgkmcnt(3)\n" \
"v_mfma_f32_16x16x32_bf16 v[52:55], a[56:59], v[44:47], v[52:55]\n" \
"s_waitcnt lgkmcnt(2)\n" \
"v_mfma_f32_16x16x32_bf16 v[60:63], a[56:59], v[48:51], v[60:63]\n" \
"ds_read_b128 v[36:39], v127 offset:0\n" \
"v_mfma_f32_16x16x32_bf16 v[56:59], a[120:123], v[44:47], v[56:59]\n" \
"v_mfma_f32_16x16x32_bf16 v[64:67], a[120:123], v[48:51], v[64:67]\n" \
"ds_read_b128 v[40:43], v127 offset:16384\n" \
"v_mfma_f32_16x16x32_bf16 v[68:71], v[44:47], a[184:187], v[68:71]\n" \
"v_mfma_f32_16x16x32_bf16 v[72:75], v[48:51], a[184:187], v[72:75]\n" \
"s_waitcnt lgkmcnt(3)\n" \
"v_mfma_f32_16x16x32_bf16 v[52:55], a[60:63], v[28:31], v[52:55]\n" \
"s_waitcnt lgkmcnt(2)\n" \
"v_mfma_f32_16x16x32_bf16 v[60:63], a[60:63], v[32:35], v[60:63]\n" \
"ds_read_b128 v[44:47], v127 offset:1024\n" \
"v_mfma_f32_16x16x32_bf16 v[56:59], a[124:127], v[28:31], v[56:59]\n" \
"v_mfma_f32_16x16x32_bf16 v[64:67], a[124:127], v[32:35], v[64:67]\n" \
"ds_read_b128 v[48:51], v127 offset:17408\n" \
"v_mfma_f32_16x16x32_bf16 v[68:71], v[28:31], a[188:191], v[68:71]\n" \
"v_mfma_f32_16x16x32_bf16 v[72:75], v[32:35], a[188:191], v[72:75]\n" \
"s_waitcnt lgkmcnt(3)\n" \
"v_mfma_f32_16x16x32_bf16 v[76:79], v[36:39], a[192:195], 0\n" \
"s_and_b32 s43, s56, 16\n" \
"s_lshl_b32 s43, s43, 10\n" \
"s_xor_b32 s43, s43, s39\n" \
"s_lshl_b32 s55, s56, 15\n" \
"s_add_u32 s55, s55, s43\n" \
"s_waitcnt lgkmcnt(2)\n" \
"v_mfma_f32_16x16x32_bf16 v[80:83], v[40:43], a[192:195], 0\n" \
"ds_read_b128 v[28:31], v127 offset:2048\n" \
"v_mfma_f32_16x16x32_bf16 v[84:87], v[128:131], v[36:39], 0\n" \
"v_mul_f32 v52, s45, v52\n" \
"v_mfma_f32_16x16x32_bf16 v[92:95], v[128:131], v[40:43], 0\n" \
"ds_read_b128 v[32:35], v127 offset:18432\n" \
"v_mfma_f32_16x16x32_bf16 v[88:91], v[192:195], v[36:39], 0\n" \
"v_mul_f32 v53, s45, v53\n" \
"buffer_load_dwordx4 v[100:103], v20, s[64:67], s55 offen offset:0 sc1\n" \
"v_mfma_f32_16x16x32_bf16 v[96:99], v[192:195], v[40:43], 0\n" \
"v_mul_f32 v54, s45, v54\n" \
"buffer_load_dwordx4 v[104:107], v20, s[64:67], s55 offen offset:1024 sc1\n" \
"s_waitcnt lgkmcnt(3)\n" \
"v_mfma_f32_16x16x32_bf16 v[76:79], v[44:47], a[196:199], v[76:79]\n" \
"v_mul_f32 v55, s45, v55\n" \
"s_waitcnt lgkmcnt(2)\n" \
"v_mfma_f32_16x16x32_bf16 v[80:83], v[48:51], a[196:199], v[80:83]\n" \
"ds_read_b128 v[36:39], v127 offset:3072\n" \
"v_mfma_f32_16x16x32_bf16 v[84:87], v[132:135], v[44:47], v[84:87]\n" \
"v_mul_f32 v56, s45, v56\n" \
"v_mfma_f32_16x16x32_bf16 v[92:95], v[132:135], v[48:51], v[92:95]\n" \
"ds_read_b128 v[40:43], v127 offset:19456\n" \
"v_mfma_f32_16x16x32_bf16 v[88:91], v[196:199], v[44:47], v[88:91]\n" \
"v_mul_f32 v57, s45, v57\n" \
"v_mfma_f32_16x16x32_bf16 v[96:99], v[196:199], v[48:51], v[96:99]\n" \
"v_mul_f32 v58, s45, v58\n" \
"s_waitcnt lgkmcnt(3)\n" \
"v_mfma_f32_16x16x32_bf16 v[76:79], v[28:31], a[200:203], v[76:79]\n" \
"v_mul_f32 v59, s45, v59\n" \
"s_waitcnt lgkmcnt(2)\n" \
"v_mfma_f32_16x16x32_bf16 v[80:83], v[32:35], a[200:203], v[80:83]\n" \
"ds_read_b128 v[44:47], v127 offset:4096\n" \
"v_mfma_f32_16x16x32_bf16 v[84:87], v[136:139], v[28:31], v[84:87]\n" \
"v_cvt_pk_bf16_f32 v52, v52, v53\n" \
"v_mfma_f32_16x16x32_bf16 v[92:95], v[136:139], v[32:35], v[92:95]\n" \
"ds_read_b128 v[48:51], v127 offset:20480\n" \
"v_mfma_f32_16x16x32_bf16 v[88:91], v[200:203], v[28:31], v[88:91]\n" \
"v_cvt_pk_bf16_f32 v53, v54, v55\n" \
"v_mfma_f32_16x16x32_bf16 v[96:99], v[200:203], v[32:35], v[96:99]\n" \
"v_cvt_pk_bf16_f32 v54, v56, v57\n" \
"s_waitcnt lgkmcnt(3)\n" \
"v_mfma_f32_16x16x32_bf16 v[76:79], v[36:39], a[204:207], v[76:79]\n" \
"v_cvt_pk_bf16_f32 v55, v58, v59\n" \
"s_waitcnt lgkmcnt(2)\n" \
"v_mfma_f32_16x16x32_bf16 v[80:83], v[40:43], a[204:207], v[80:83]\n" \
"ds_read_b128 v[28:31], v127 offset:5120\n" \
"v_mfma_f32_16x16x32_bf16 v[84:87], v[140:143], v[36:39], v[84:87]\n" \
"buffer_store_dwordx4 v[52:55], v21, s[64:67], s51 offen offset:0 sc1\n" \
"v_mfma_f32_16x16x32_bf16 v[92:95], v[140:143], v[40:43], v[92:95]\n" \
"ds_read_b128 v[32:35], v127 offset:21504\n" \
"v_mfma_f32_16x16x32_bf16 v[88:91], v[204:207], v[36:39], v[88:91]\n" \
"v_mul_f32 v60, s45, v60\n" \
"v_mfma_f32_16x16x32_bf16 v[96:99], v[204:207], v[40:43], v[96:99]\n" \
"v_mul_f32 v61, s45, v61\n" \
"s_waitcnt lgkmcnt(3)\n" \
"v_mfma_f32_16x16x32_bf16 v[76:79], v[44:47], a[208:211], v[76:79]\n" \
"s_barrier\n" \
"ds_write_b128 v23, v[52:55] offset:0\n" \
"s_waitcnt lgkmcnt(3)\n" \
"v_mfma_f32_16x16x32_bf16 v[80:83], v[48:51], a[208:211], v[80:83]\n" \
"ds_read_b128 v[36:39], v127 offset:6144\n" \
"v_mfma_f32_16x16x32_bf16 v[84:87], v[144:147], v[44:47], v[84:87]\n" \
"v_mul_f32 v62, s45, v62\n" \
"v_mfma_f32_16x16x32_bf16 v[92:95], v[144:147], v[48:51], v[92:95]\n" \
"ds_read_b128 v[40:43], v127 offset:22528\n" \
"v_mfma_f32_16x16x32_bf16 v[88:91], v[208:211], v[44:47], v[88:91]\n" \
"v_mul_f32 v63, s45, v63\n" \
"v_mfma_f32_16x16x32_bf16 v[96:99], v[208:211], v[48:51], v[96:99]\n" \
"v_mul_f32 v64, s45, v64\n" \
"s_waitcnt lgkmcnt(4)\n" \
"v_mfma_f32_16x16x32_bf16 v[76:79], v[28:31], a[212:215], v[76:79]\n" \
"v_mul_f32 v65, s45, v65\n" \
"s_waitcnt lgkmcnt(3)\n" \
"v_mfma_f32_16x16x32_bf16 v[80:83], v[32:35], a[212:215], v[80:83]\n" \
"ds_read_b128 v[44:47], v127 offset:7168\n" \
"v_mfma_f32_16x16x32_bf16 v[84:87], v[148:151], v[28:31], v[84:87]\n" \
"v_mul_f32 v66, s45, v66\n" \
"v_mfma_f32_16x16x32_bf16 v[92:95], v[148:151], v[32:35], v[92:95]\n" \
"ds_read_b128 v[48:51], v127 offset:23552\n" \
"v_mfma_f32_16x16x32_bf16 v[88:91], v[212:215], v[28:31], v[88:91]\n" \
"v_mul_f32 v67, s45, v67\n" \
"v_mfma_f32_16x16x32_bf16 v[96:99], v[212:215], v[32:35], v[96:99]\n" \
"v_cvt_pk_bf16_f32 v60, v60, v61\n" \
"s_waitcnt lgkmcnt(3)\n" \
"v_mfma_f32_16x16x32_bf16 v[76:79], v[36:39], a[216:219], v[76:79]\n" \
"v_cvt_pk_bf16_f32 v61, v62, v63\n" \
"s_waitcnt lgkmcnt(2)\n" \
"v_mfma_f32_16x16x32_bf16 v[80:83], v[40:43], a[216:219], v[80:83]\n" \
"ds_read_b128 v[28:31], v127 offset:8192\n" \
"v_mfma_f32_16x16x32_bf16 v[84:87], v[152:155], v[36:39], v[84:87]\n" \
"v_cvt_pk_bf16_f32 v62, v64, v65\n" \
"v_mfma_f32_16x16x32_bf16 v[92:95], v[152:155], v[40:43], v[92:95]\n" \
"ds_read_b128 v[32:35], v127 offset:24576\n" \
"v_mfma_f32_16x16x32_bf16 v[88:91], v[216:219], v[36:39], v[88:91]\n" \
"v_cvt_pk_bf16_f32 v63, v66, v67\n" \
"v_mfma_f32_16x16x32_bf16 v[96:99], v[216:219], v[40:43], v[96:99]\n" \
"s_add_u32 s43, s51, 4096\n" \
"buffer_store_dwordx4 v[60:63], v21, s[64:67], s43 offen sc1\n" \
"s_waitcnt lgkmcnt(3)\n" \
"v_mfma_f32_16x16x32_bf16 v[76:79], v[44:47], a[220:223], v[76:79]\n" \
"ds_write_b128 v23, v[60:63] offset:4096\n" \
"s_waitcnt lgkmcnt(3)\n" \
"v_mfma_f32_16x16x32_bf16 v[80:83], v[48:51], a[220:223], v[80:83]\n" \
"ds_read_b128 v[36:39], v127 offset:9216\n" \
"v_mfma_f32_16x16x32_bf16 v[84:87], v[156:159], v[44:47], v[84:87]\n" \
"v_cvt_pk_bf16_f32 v68, v68, v69\n" \
"v_mfma_f32_16x16x32_bf16 v[92:95], v[156:159], v[48:51], v[92:95]\n" \
"ds_read_b128 v[40:43], v127 offset:25600\n" \
"v_mfma_f32_16x16x32_bf16 v[88:91], v[220:223], v[44:47], v[88:91]\n" \
"v_cvt_pk_bf16_f32 v69, v70, v71\n" \
"v_mfma_f32_16x16x32_bf16 v[96:99], v[220:223], v[48:51], v[96:99]\n" \
"v_cvt_pk_bf16_f32 v70, v72, v73\n" \
"s_waitcnt lgkmcnt(4)\n" \
"v_mfma_f32_16x16x32_bf16 v[76:79], v[28:31], a[224:227], v[76:79]\n" \
"v_cvt_pk_bf16_f32 v71, v74, v75\n" \
"s_waitcnt lgkmcnt(3)\n" \
"v_mfma_f32_16x16x32_bf16 v[80:83], v[32:35], a[224:227], v[80:83]\n" \
"ds_read_b128 v[44:47], v127 offset:10240\n" \
"v_mfma_f32_16x16x32_bf16 v[84:87], v[160:163], v[28:31], v[84:87]\n" \
"buffer_store_dwordx4 v[68:71], v22, s[64:67], s51 offen offset:0 sc1\n" \
"v_mfma_f32_16x16x32_bf16 v[92:95], v[160:163], v[32:35], v[92:95]\n" \
"ds_read_b128 v[48:51], v127 offset:26624\n" \
"v_mfma_f32_16x16x32_bf16 v[88:91], v[224:227], v[28:31], v[88:91]\n" \
"ds_write_b128 v24, v[68:71] offset:0\n" \
"v_mfma_f32_16x16x32_bf16 v[96:99], v[224:227], v[32:35], v[96:99]\n" \
"s_waitcnt vmcnt(3)\n" \
"v_max3_u32 v108, v100, v101, v102\n" \
"s_waitcnt lgkmcnt(4)\n" \
"v_mfma_f32_16x16x32_bf16 v[76:79], v[36:39], a[228:231], v[76:79]\n" \
"v_max3_u32 v109, v103, v104, v105\n" \
"s_waitcnt lgkmcnt(3)\n" \
"v_mfma_f32_16x16x32_bf16 v[80:83], v[40:43], a[228:231], v[80:83]\n" \
"ds_read_b128 v[28:31], v127 offset:11264\n" \
"v_mfma_f32_16x16x32_bf16 v[84:87], v[164:167], v[36:39], v[84:87]\n" \
"v_max3_u32 v108, v108, v106, v107\n" \
"v_mfma_f32_16x16x32_bf16 v[92:95], v[164:167], v[40:43], v[92:95]\n" \
"ds_read_b128 v[32:35], v127 offset:27648\n" \
"v_mfma_f32_16x16x32_bf16 v[88:91], v[228:231], v[36:39], v[88:91]\n" \
"v_max_u32 v108, v108, v109\n" \
"v_mfma_f32_16x16x32_bf16 v[96:99], v[228:231], v[40:43], v[96:99]\n" \
"v_cmp_eq_u32 vcc, -1, v108\n" \
"s_waitcnt lgkmcnt(4)\n" \
"v_mfma_f32_16x16x32_bf16 v[76:79], v[44:47], a[232:235], v[76:79]\n" \
"v_perm_b32 v108, v102, v100, s46\n" \
"s_waitcnt lgkmcnt(3)\n" \
"v_mfma_f32_16x16x32_bf16 v[80:83], v[48:51], a[232:235], v[80:83]\n" \
"ds_read_b128 v[36:39], v127 offset:12288\n" \
"v_mfma_f32_16x16x32_bf16 v[84:87], v[168:171], v[44:47], v[84:87]\n" \
"v_perm_b32 v109, v102, v100, s48\n" \
"v_mfma_f32_16x16x32_bf16 v[92:95], v[168:171], v[48:51], v[92:95]\n" \
"ds_read_b128 v[40:43], v127 offset:28672\n" \
"v_mfma_f32_16x16x32_bf16 v[88:91], v[232:235], v[44:47], v[88:91]\n" \
"s_or_b32 s59, vcc_lo, vcc_hi\n" \
"v_perm_b32 v110, v103, v101, s46\n" \
"v_mfma_f32_16x16x32_bf16 v[96:99], v[232:235], v[48:51], v[96:99]\n" \
"v_perm_b32 v111, v103, v101, s48\n" \
"s_waitcnt lgkmcnt(3)\n" \
"v_mfma_f32_16x16x32_bf16 v[76:79], v[28:31], a[236:239], v[76:79]\n" \
"v_perm_b32 v112, v106, v104, s46\n" \
"s_waitcnt lgkmcnt(2)\n" \
"v_mfma_f32_16x16x32_bf16 v[80:83], v[32:35], a[236:239], v[80:83]\n" \
"ds_read_b128 v[44:47], v127 offset:13312\n" \
"v_mfma_f32_16x16x32_bf16 v[84:87], v[172:175], v[28:31], v[84:87]\n" \
"v_perm_b32 v113, v106, v104, s48\n" \
"v_mfma_f32_16x16x32_bf16 v[92:95], v[172:175], v[32:35], v[92:95]\n" \
"ds_read_b128 v[48:51], v127 offset:29696\n" \
"v_mfma_f32_16x16x32_bf16 v[88:91], v[236:239], v[28:31], v[88:91]\n" \
"v_perm_b32 v114, v107, v105, s46\n" \
"v_mfma_f32_16x16x32_bf16 v[96:99], v[236:239], v[32:35], v[96:99]\n" \
"v_perm_b32 v115, v107, v105, s48\n" \
"s_waitcnt lgkmcnt(3)\n" \
"v_mfma_f32_16x16x32_bf16 v[76:79], v[36:39], a[240:243], v[76:79]\n" \
"ds_write2st64_b32 v3, v108, v109 offset0:0 offset1:1\n" \
"s_waitcnt lgkmcnt(3)\n" \
"v_mfma_f32_16x16x32_bf16 v[80:83], v[40:43], a[240:243], v[80:83]\n" \
"ds_read_b128 v[28:31], v127 offset:14336\n" \
"v_mfma_f32_16x16x32_bf16 v[84:87], v[176:179], v[36:39], v[84:87]\n" \
"ds_write2st64_b32 v3, v110, v111 offset0:2 offset1:3\n" \
"v_mfma_f32_16x16x32_bf16 v[92:95], v[176:179], v[40:43], v[92:95]\n" \
"ds_read_b128 v[32:35], v127 offset:30720\n" \
"v_mfma_f32_16x16x32_bf16 v[88:91], v[240:243], v[36:39], v[88:91]\n" \
"ds_write2st64_b32 v3, v112, v113 offset0:4 offset1:5\n" \
"v_mfma_f32_16x16x32_bf16 v[96:99], v[240:243], v[40:43], v[96:99]\n" \
"ds_write2st64_b32 v3, v114, v115 offset0:6 offset1:7\n" \
"s_waitcnt lgkmcnt(7)\n" \
"v_mfma_f32_16x16x32_bf16 v[76:79], v[44:47], a[244:247], v[76:79]\n" \
"s_waitcnt lgkmcnt(6)\n" \
"v_mfma_f32_16x16x32_bf16 v[80:83], v[48:51], a[244:247], v[80:83]\n" \
"ds_read_b128 v[36:39], v127 offset:15360\n" \
"v_mfma_f32_16x16x32_bf16 v[84:87], v[180:183], v[44:47], v[84:87]\n" \
"v_mfma_f32_16x16x32_bf16 v[92:95], v[180:183], v[48:51], v[92:95]\n" \
"ds_read_b128 v[40:43], v127 offset:31744\n" \
"v_mfma_f32_16x16x32_bf16 v[88:91], v[244:247], v[44:47], v[88:91]\n" \
"v_mfma_f32_16x16x32_bf16 v[96:99], v[244:247], v[48:51], v[96:99]\n" \
"s_waitcnt lgkmcnt(6)\n" \
"v_mfma_f32_16x16x32_bf16 v[76:79], v[28:31], a[248:251], v[76:79]\n" \
"s_waitcnt lgkmcnt(4)\n" \
"v_mfma_f32_16x16x32_bf16 v[80:83], v[32:35], a[248:251], v[80:83]\n" \
"v_mfma_f32_16x16x32_bf16 v[84:87], v[184:187], v[28:31], v[84:87]\n" \
"v_mfma_f32_16x16x32_bf16 v[92:95], v[184:187], v[32:35], v[92:95]\n" \
"v_mfma_f32_16x16x32_bf16 v[88:91], v[248:251], v[28:31], v[88:91]\n" \
"v_mfma_f32_16x16x32_bf16 v[96:99], v[248:251], v[32:35], v[96:99]\n" \
"s_waitcnt lgkmcnt(1)\n" \
"v_mfma_f32_16x16x32_bf16 v[76:79], v[36:39], a[252:255], v[76:79]\n" \
"s_waitcnt lgkmcnt(0)\n" \
"v_mfma_f32_16x16x32_bf16 v[80:83], v[40:43], a[252:255], v[80:83]\n" \
"v_mfma_f32_16x16x32_bf16 v[84:87], v[188:191], v[36:39], v[84:87]\n" \
"v_mfma_f32_16x16x32_bf16 v[92:95], v[188:191], v[40:43], v[92:95]\n" \
"v_cvt_pk_bf16_f32 v76, v76, v77\n" \
"v_mfma_f32_16x16x32_bf16 v[88:91], v[252:255], v[36:39], v[88:91]\n" \
"v_cvt_pk_bf16_f32 v77, v78, v79\n" \
"v_cvt_pk_bf16_f32 v78, v80, v81\n" \
"v_mfma_f32_16x16x32_bf16 v[96:99], v[252:255], v[40:43], v[96:99]\n" \
"v_cvt_pk_bf16_f32 v79, v82, v83\n" \
"buffer_store_dwordx4 v[76:79], v22, s[64:67], s51 offen offset:1024 sc1\n" \
"ds_write_b128 v24, v[76:79] offset:1024\n" \
"s_lshl_b32 s43, s40, 1\n" \
"s_add_u32 s43, s43, s38\n" \
"s_add_u32 s43, s43, 1\n" \
"v_mov_b32 v124, s43\n" \
"s_add_u32 s41, s41, 24576\n" \
"s_cmp_eq_u32 s41, 163840\n" \
"s_cselect_b32 s41, 65536, s41\n" \
"s_add_u32 s51, s51, 1048576\n" \
"v_xor_b32 v127, 32768, v127\n" \
"s_cmp_ge_u32 s40, 3\n" \
"s_cbranch_scc1 .Lx_a0_%=\n" \
"v_cvt_pk_bf16_f32 v84, v84, v85\n" \
"v_cvt_pk_bf16_f32 v85, v86, v87\n" \
"v_cvt_pk_bf16_f32 v86, v88, v89\n" \
"v_cvt_pk_bf16_f32 v87, v90, v91\n" \
"ds_write_b128 v23, v[84:87] offset:16384\n" \
"v_cvt_pk_bf16_f32 v92, v92, v93\n" \
"v_cvt_pk_bf16_f32 v93, v94, v95\n" \
"v_cvt_pk_bf16_f32 v94, v96, v97\n" \
"v_cvt_pk_bf16_f32 v95, v98, v99\n" \
"ds_write_b128 v23, v[92:95] offset:20480\n" \
"s_waitcnt vmcnt(0) lgkmcnt(0)\n" \
"s_barrier\n" \
"ds_read_b128 v[28:31], v127 offset:0\n" \
"ds_read_b128 v[32:35], v127 offset:16384\n" \
"ds_read_b128 v[36:39], v127 offset:1024\n" \
"ds_read_b128 v[40:43], v127 offset:17408\n" \
"s_cmp_lg_u32 s36, 0\n" \
"s_cbranch_scc1 .Lx_next_%=\n" \
"s_mov_b64 exec, 1\n" \
"buffer_store_dword v124, off, s[64:67], s52 sc1\n" \
"s_mov_b64 exec, -1\n" \
"s_branch .Lx_next_%=\n" \
".Lx_att_%=:\n" \
"s_and_b32 s43, s56, 16\n" \
"s_lshl_b32 s43, s43, 10\n" \
"s_xor_b32 s43, s43, s39\n" \
"s_lshl_b32 s55, s56, 15\n" \
"s_add_u32 s55, s55, s43\n" \
"buffer_load_dwordx4 v[100:103], v20, s[64:67], s55 offen offset:0 sc1\n" \
"buffer_load_dwordx4 v[104:107], v20, s[64:67], s55 offen offset:1024 sc1\n" \
"s_mov_b32 s59, 1\n" \
".Lx_a0_%=:\n" \
"s_cmp_eq_u32 s56, 0\n" \
"s_cbranch_scc1 .Lx_m0_%=\n" \
".Lx_a1_%=:\n" \
"s_and_b32 s43, s36, 1\n" \
"s_lshl_b32 s43, s43, 12\n" \
"s_add_u32 s43, s43, s42\n" \
"s_lshr_b32 s44, s36, 1\n" \
"s_lshl_b32 s44, s44, 12\n" \
"s_add_u32 s44, s44, s42\n" \
"v_add_u32 v116, s42, v0\n" \
"v_add_u32 v24, s43, v0\n" \
"ds_read_b128 v[28:31], v24 offset:16384\n" \
"ds_read_b128 v[44:47], v116 offset:0\n" \
"ds_read_b128 v[60:63], v116 offset:4096\n" \
"ds_read_b128 v[32:35], v24 offset:17408\n" \
"ds_read_b128 v[48:51], v116 offset:1024\n" \
"ds_read_b128 v[64:67], v116 offset:5120\n" \
"ds_read_b128 v[36:39], v24 offset:18432\n" \
"ds_read_b128 v[52:55], v116 offset:2048\n" \
"ds_read_b128 v[68:71], v116 offset:6144\n" \
"ds_read_b128 v[40:43], v24 offset:19456\n" \
"ds_read_b128 v[56:59], v116 offset:3072\n" \
"ds_read_b128 v[72:75], v116 offset:7168\n" \
"v_add_u32 v116, s44, v0\n" \
"s_cmp_ge_u32 s40, 32\n" \
"s_cbranch_scc1 .Lx_nq_%=\n" \
"v_cvt_pk_bf16_f32 v84, v84, v85\n" \
"v_cvt_pk_bf16_f32 v85, v86, v87\n" \
"v_cvt_pk_bf16_f32 v86, v88, v89\n" \
"v_cvt_pk_bf16_f32 v87, v90, v91\n" \
"v_cvt_pk_bf16_f32 v92, v92, v93\n" \
"v_cvt_pk_bf16_f32 v93, v94, v95\n" \
"v_cvt_pk_bf16_f32 v94, v96, v97\n" \
"v_cvt_pk_bf16_f32 v95, v98, v99\n" \
".Lx_nq_%=:\n" \
"s_waitcnt lgkmcnt(10)\n" \
"v_mfma_f32_16x16x32_bf16 v[76:79], v[44:47], v[28:31], v[12:15]\n" \
"s_waitcnt lgkmcnt(9)\n" \
"v_mfma_f32_16x16x32_bf16 v[80:83], v[60:63], v[28:31], v[16:19]\n" \
"s_waitcnt lgkmcnt(7)\n" \
"v_mfma_f32_16x16x32_bf16 v[76:79], v[48:51], v[32:35], v[76:79]\n" \
"s_waitcnt lgkmcnt(6)\n" \
"v_mfma_f32_16x16x32_bf16 v[80:83], v[64:67], v[32:35], v[80:83]\n" \
"s_waitcnt lgkmcnt(4)\n" \
"v_mfma_f32_16x16x32_bf16 v[76:79], v[52:55], v[36:39], v[76:79]\n" \
"s_waitcnt lgkmcnt(3)\n" \
"v_mfma_f32_16x16x32_bf16 v[80:83], v[68:71], v[36:39], v[80:83]\n" \
"s_waitcnt lgkmcnt(1)\n" \
"v_mfma_f32_16x16x32_bf16 v[76:79], v[56:59], v[40:43], v[76:79]\n" \
"s_waitcnt lgkmcnt(0)\n" \
"v_mfma_f32_16x16x32_bf16 v[80:83], v[72:75], v[40:43], v[80:83]\n" \
"s_cbranch_scc1 .Lx_nw_%=\n" \
"ds_write_b128 v23, v[84:87] offset:16384\n" \
"ds_write_b128 v23, v[92:95] offset:20480\n" \
".Lx_nw_%=:\n" \
"v_mov_b32 v27, 0\n" \
"v_add_u32 v24, s58, v0\n" \
"s_cmp_lg_u32 s59, 0\n" \
"s_cbranch_scc1 .Lx_slow_%=\n" \
".Lx_join_%=:\n" \
"s_waitcnt lgkmcnt(0)\n" \
"s_barrier\n" \
"ds_read_b128 v[44:47], v24 offset:0\n" \
"ds_read_b128 v[48:51], v24 offset:1024\n" \
"ds_read_b128 v[52:55], v24 offset:2048\n" \
"ds_read_b128 v[56:59], v24 offset:3072\n" \
"ds_read_b128 v[60:63], v24 offset:16384\n" \
"ds_read_b128 v[64:67], v24 offset:17408\n" \
"ds_read_b128 v[68:71], v24 offset:18432\n" \
"ds_read_b128 v[72:75], v24 offset:19456\n" \
"v_mov_b64 v[100:101], 0\n" \
"v_mov_b64 v[102:103], 0\n" \
"v_mov_b64 v[104:105], 0\n" \
"v_mov_b64 v[106:107], 0\n" \
"v_mov_b64 v[108:109], 0\n" \
"v_mov_b64 v[110:111], 0\n" \
"v_mov_b64 v[112:113], 0\n" \
"v_mov_b64 v[114:115], 0\n" \
"v_max3_f32 v23, v76, v77, v78\n" \
"v_max3_f32 v23, v23, v79, v80\n" \
"v_max3_f32 v23, v23, v81, v82\n" \
"v_max_f32 v23, v23, v83\n" \
"s_waitcnt lgkmcnt(7)\n" \
"v_smfmac_f32_16x16x64_bf16 v[100:103], v[44:47], a[0:7], v2\n" \
"v_smfmac_f32_16x16x64_bf16 v[104:107], v[44:47], a[64:71], v2\n" \
"s_waitcnt lgkmcnt(6)\n" \
"v_smfmac_f32_16x16x64_bf16 v[100:103], v[48:51], a[8:15], v2\n" \
"v_smfmac_f32_16x16x64_bf16 v[104:107], v[48:51], a[72:79], v2\n" \
"s_waitcnt lgkmcnt(5)\n" \
"v_smfmac_f32_16x16x64_bf16 v[100:103], v[52:55], a[16:23], v2\n" \
"v_smfmac_f32_16x16x64_bf16 v[104:107], v[52:55], a[80:87], v2\n" \
"s_waitcnt lgkmcnt(4)\n" \
"v_smfmac_f32_16x16x64_bf16 v[100:103], v[56:59], a[24:31], v2\n" \
"v_smfmac_f32_16x16x64_bf16 v[104:107], v[56:59], a[88:95], v2\n" \
"s_waitcnt lgkmcnt(3)\n" \
"v_smfmac_f32_16x16x64_bf16 v[100:103], v[60:63], a[32:39], v2\n" \
"v_smfmac_f32_16x16x64_bf16 v[104:107], v[60:63], a[96:103], v2\n" \
"s_waitcnt lgkmcnt(2)\n" \
"v_smfmac_f32_16x16x64_bf16 v[100:103], v[64:67], a[40:47], v2\n" \
"v_smfmac_f32_16x16x64_bf16 v[104:107], v[64:67], a[104:111], v2\n" \
"s_waitcnt lgkmcnt(1)\n" \
"v_smfmac_f32_16x16x64_bf16 v[100:103], v[68:71], a[48:55], v2\n" \
"v_smfmac_f32_16x16x64_bf16 v[104:107], v[68:71], a[112:119], v2\n" \
"s_waitcnt lgkmcnt(0)\n" \
"v_smfmac_f32_16x16x64_bf16 v[100:103], v[72:75], a[56:63], v2\n" \
"v_smfmac_f32_16x16x64_bf16 v[104:107], v[72:75], a[120:127], v2\n" \
"v_smfmac_f32_16x16x64_bf16 v[108:111], v[44:47], a[128:135], v2\n" \
"ds_read_b128 v[84:87], v116 offset:8192\n" \
"v_smfmac_f32_16x16x64_bf16 v[112:115], v[44:47], a[192:199], v2\n" \
"ds_read_b128 v[88:91], v116 offset:9216\n" \
"v_add_f32 v100, v100, v102\n" \
"v_smfmac_f32_16x16x64_bf16 v[108:111], v[48:51], a[136:143], v2\n" \
"ds_read_b128 v[92:95], v116 offset:10240\n" \
"v_add_f32 v101, v101, v103\n" \
"v_smfmac_f32_16x16x64_bf16 v[112:115], v[48:51], a[200:207], v2\n" \
"ds_read_b128 v[96:99], v116 offset:11264\n" \
"v_add_f32 v104, v104, v106\n" \
"v_smfmac_f32_16x16x64_bf16 v[108:111], v[52:55], a[144:151], v2\n" \
"v_add_f32 v105, v105, v107\n" \
"v_smfmac_f32_16x16x64_bf16 v[112:115], v[52:55], a[208:215], v2\n" \
"v_mul_f32 v100, s45, v100\n" \
"v_smfmac_f32_16x16x64_bf16 v[108:111], v[56:59], a[152:159], v2\n" \
"v_mul_f32 v101, s45, v101\n" \
"v_smfmac_f32_16x16x64_bf16 v[112:115], v[56:59], a[216:223], v2\n" \
"v_mul_f32 v104, s45, v104\n" \
"v_smfmac_f32_16x16x64_bf16 v[108:111], v[60:63], a[160:167], v2\n" \
"v_mul_f32 v105, s45, v105\n" \
"v_smfmac_f32_16x16x64_bf16 v[112:115], v[60:63], a[224:231], v2\n" \
"v_cvt_pk_bf16_f32 v100, v100, v101\n" \
"v_smfmac_f32_16x16x64_bf16 v[108:111], v[64:67], a[168:175], v2\n" \
"v_cvt_pk_bf16_f32 v104, v104, v105\n" \
"v_smfmac_f32_16x16x64_bf16 v[112:115], v[64:67], a[232:239], v2\n" \
"ds_write2st64_b32 v4, v100, v104 offset0:0 offset1:2\n" \
"v_smfmac_f32_16x16x64_bf16 v[108:111], v[68:71], a[176:183], v2\n" \
"v_smfmac_f32_16x16x64_bf16 v[112:115], v[68:71], a[240:247], v2\n" \
"v_smfmac_f32_16x16x64_bf16 v[108:111], v[72:75], a[184:191], v2\n" \
"s_waitcnt lgkmcnt(0)\n" \
"s_waitcnt vmcnt(0)\n" \
"s_barrier\n" \
"v_smfmac_f32_16x16x64_bf16 v[112:115], v[72:75], a[248:255], v2\n" \
"ds_read_b64_tr_b16 v[44:45], v5 offset:0\n" \
"ds_read_b64_tr_b16 v[46:47], v5 offset:512\n" \
"ds_read_b64_tr_b16 v[48:49], v5 offset:1024\n" \
"ds_read_b64_tr_b16 v[50:51], v5 offset:1536\n" \
"ds_read_b64_tr_b16 v[52:53], v5 offset:2048\n" \
"ds_read_b64_tr_b16 v[54:55], v5 offset:2560\n" \
"ds_read_b64_tr_b16 v[56:57], v5 offset:3072\n" \
"ds_read_b64_tr_b16 v[58:59], v5 offset:3584\n" \
"s_cmp_lg_u32 s36, 0\n" \
"s_cbranch_scc1 .Lx_nf_%=\n" \
"s_mov_b64 exec, 1\n" \
"buffer_store_dword v124, off, s[64:67], s52 sc1\n" \
"s_mov_b64 exec, -1\n" \
".Lx_nf_%=:\n" \
"s_nop 1\n" \
"v_add_f32 v108, v108, v110\n" \
"v_add_f32 v109, v109, v111\n" \
"s_nop 0\n" \
"v_add_f32 v112, v112, v114\n" \
"v_add_f32 v113, v113, v115\n" \
"v_cvt_pk_bf16_f32 v108, v108, v109\n" \
"v_cvt_pk_bf16_f32 v112, v112, v113\n" \
"v_add_u32 v24, 4, v4\n" \
"ds_write2st64_b32 v24, v108, v112 offset0:0 offset1:2\n" \
"s_waitcnt lgkmcnt(7)\n" \
"v_mfma_f32_16x16x32_bf16 v[100:103], v[44:47], v[28:31], 0\n" \
"s_waitcnt lgkmcnt(5)\n" \
"v_mfma_f32_16x16x32_bf16 v[100:103], v[48:51], v[32:35], v[100:103]\n" \
"s_waitcnt lgkmcnt(3)\n" \
"v_mfma_f32_16x16x32_bf16 v[100:103], v[52:55], v[36:39], v[100:103]\n" \
"s_waitcnt lgkmcnt(1)\n" \
"v_mfma_f32_16x16x32_bf16 v[100:103], v[56:59], v[40:43], v[100:103]\n" \
"s_waitcnt lgkmcnt(0)\n" \
"s_barrier\n" \
"ds_read_b32 v110, v6 offset:0\n" \
"ds_read_b32 v114, v6 offset:512\n" \
"ds_read_b32 v118, v6 offset:1024\n" \
"ds_read_b32 v122, v6 offset:1536\n" \
"v_mov_b32 v111, 0\n" \
"v_mov_b32 v115, 0\n" \
"v_mov_b32 v119, 0\n" \
"v_mov_b32 v123, 0\n" \
"v_max3_f32 v23, v23, v100, v101\n" \
"v_mov_b32 v24, v23\n" \
"s_nop 1\n" \
"v_permlane32_swap_b32 v23, v24\n" \
"s_nop 1\n" \
"v_max_f32 v23, v23, v24\n" \
"v_mov_b32 v24, v23\n" \
"s_nop 1\n" \
"v_permlane16_swap_b32 v23, v24\n" \
"s_nop 1\n" \
"v_max_f32 v23, v23, v24\n" \
"v_sub_f32 v76, v76, v23\n" \
"v_sub_f32 v77, v77, v23\n" \
"v_sub_f32 v78, v78, v23\n" \
"v_sub_f32 v79, v79, v23\n" \
"v_sub_f32 v80, v80, v23\n" \
"v_sub_f32 v81, v81, v23\n" \
"v_sub_f32 v82, v82, v23\n" \
"v_sub_f32 v83, v83, v23\n" \
"v_sub_f32 v100, v100, v23\n" \
"v_sub_f32 v101, v101, v23\n" \
"v_exp_f32 v76, v76\n" \
"v_exp_f32 v77, v77\n" \
"v_exp_f32 v78, v78\n" \
"v_exp_f32 v79, v79\n" \
"v_exp_f32 v80, v80\n" \
"v_exp_f32 v81, v81\n" \
"v_exp_f32 v82, v82\n" \
"v_exp_f32 v83, v83\n" \
"v_cvt_pk_bf16_f32 v76, v76, v77\n" \
"v_cvt_pk_bf16_f32 v77, v78, v79\n" \
"v_exp_f32 v100, v100\n" \
"v_exp_f32 v101, v101\n" \
"v_cvt_pk_bf16_f32 v78, v80, v81\n" \
"v_cvt_pk_bf16_f32 v79, v82, v83\n" \
"v_cvt_pk_bf16_f32 v26, v100, v101\n" \
"s_nop 0\n" \
"v_mfma_f32_16x16x32_bf16 v[100:103], v[8:11], v[76:79], 0\n" \
"v_mfma_f32_16x16x16_bf16 v[104:107], v[8:9], v[26:27], 0\n" \
"v_mfma_f32_16x16x32_bf16 v[44:47], v[84:87], v[76:79], 0\n" \
"v_mfma_f32_16x16x32_bf16 v[48:51], v[88:91], v[76:79], 0\n" \
"v_mfma_f32_16x16x32_bf16 v[52:55], v[92:95], v[76:79], 0\n" \
"v_add_f32 v100, v100, v104\n" \
"v_mfma_f32_16x16x32_bf16 v[56:59], v[96:99], v[76:79], 0\n" \
"v_rcp_f32 v100, v100\n" \
"s_waitcnt lgkmcnt(3)\n" \
"v_mfma_f32_16x16x16_bf16 v[60:63], v[110:111], v[26:27], 0\n" \
"s_waitcnt lgkmcnt(2)\n" \
"v_mfma_f32_16x16x16_bf16 v[64:67], v[114:115], v[26:27], 0\n" \
"v_mov_b32 v101, v100\n" \
"s_waitcnt lgkmcnt(1)\n" \
"v_mfma_f32_16x16x16_bf16 v[68:71], v[118:119], v[26:27], 0\n" \
"s_waitcnt lgkmcnt(0)\n" \
"v_mfma_f32_16x16x16_bf16 v[72:75], v[122:123], v[26:27], 0\n" \
"s_add_u32 s43, s56, 1\n" \
"v_mov_b32 v125, s43\n" \
"s_add_u32 s56, s56, 2\n" \
"ds_read_b128 v[28:31], v127 offset:0\n" \
"ds_read_b128 v[32:35], v127 offset:16384\n" \
"ds_read_b128 v[36:39], v127 offset:1024\n" \
"ds_read_b128 v[40:43], v127 offset:17408\n" \
"v_pk_add_f32 v[44:45], v[44:45], v[60:61]\n" \
"v_pk_add_f32 v[46:47], v[46:47], v[62:63]\n" \
"v_pk_add_f32 v[48:49], v[48:49], v[64:65]\n" \
"v_pk_add_f32 v[50:51], v[50:51], v[66:67]\n" \
"v_pk_mul_f32 v[44:45], v[44:45], v[100:101]\n" \
"v_pk_mul_f32 v[46:47], v[46:47], v[100:101]\n" \
"v_pk_mul_f32 v[48:49], v[48:49], v[100:101]\n" \
"v_pk_mul_f32 v[50:51], v[50:51], v[100:101]\n" \
"v_cvt_pk_bf16_f32 v44, v44, v45\n" \
"v_cvt_pk_bf16_f32 v45, v46, v47\n" \
"v_cvt_pk_bf16_f32 v46, v48, v49\n" \
"v_cvt_pk_bf16_f32 v47, v50, v51\n" \
"buffer_store_dwordx4 v[44:47], v7, s[64:67], s54 offen offset:0 sc1\n" \
"s_nop 0\n" \
"v_pk_add_f32 v[52:53], v[52:53], v[68:69]\n" \
"v_pk_add_f32 v[54:55], v[54:55], v[70:71]\n" \
"v_pk_add_f32 v[56:57], v[56:57], v[72:73]\n" \
"v_pk_add_f32 v[58:59], v[58:59], v[74:75]\n" \
"v_pk_mul_f32 v[52:53], v[52:53], v[100:101]\n" \
"v_pk_mul_f32 v[54:55], v[54:55], v[100:101]\n" \
"v_pk_mul_f32 v[56:57], v[56:57], v[100:101]\n" \
"v_pk_mul_f32 v[58:59], v[58:59], v[100:101]\n" \
"v_cvt_pk_bf16_f32 v52, v52, v53\n" \
"v_cvt_pk_bf16_f32 v53, v54, v55\n" \
"v_cvt_pk_bf16_f32 v54, v56, v57\n" \
"v_cvt_pk_bf16_f32 v55, v58, v59\n" \
"buffer_store_dwordx4 v[52:55], v7, s[64:67], s54 offen offset:128 sc1\n" \
"s_mov_b32 s43, s42\n" \
"s_add_u32 s42, s42, 24576\n" \
"s_cmp_eq_u32 s42, 163840\n" \
"s_cselect_b32 s42, 65536, s42\n" \
"s_sub_u32 s43, s42, s43\n" \
"v_add_u32 v4, s43, v4\n" \
"v_add_u32 v5, s43, v5\n" \
"v_add_u32 v6, s43, v6\n" \
"s_add_u32 s54, s54, 524288\n" \
"s_cmp_ge_u32 s40, 31\n" \
"s_cbranch_scc1 .Lx_tail_%=\n" \
".Lx_next_%=:\n" \
"s_add_u32 s40, s40, 1\n" \
"s_cmp_lt_u32 s40, 32\n" \
"s_cbranch_scc1 .Lx_top_%=\n" \
"s_cmp_lt_u32 s56, 64\n" \
"s_cbranch_scc1 .Lx_att_%=\n" \
"s_branch .Lx_end_%=\n" \
".Lx_tail_%=:\n" \
"s_waitcnt vmcnt(0)\n" \
"s_mov_b64 exec, 1\n" \
"buffer_store_dword v125, off, s[64:67], s53 sc1\n" \
"s_mov_b64 exec, -1\n" \
"s_waitcnt lgkmcnt(0)\n" \
"s_barrier\n" \
"s_branch .Lx_next_%=\n" \
".Lx_slow_%=:\n" \
"s_cmp_eq_u32 s59, 1\n" \
"s_cbranch_scc1 .Lx_fresh_%=\n" \
"buffer_load_dwordx4 v[100:103], v20, s[64:67], s55 offen offset:0 sc1\n" \
"buffer_load_dwordx4 v[104:107], v20, s[64:67], s55 offen offset:1024 sc1\n" \
".Lx_fresh_%=:\n" \
"s_waitcnt vmcnt(0)\n" \
"s_cmp_eq_u32 s56, 0\n" \
"s_cbranch_scc1 .Lx_wr_%=\n" \
".Lx_chk_%=:\n" \
"v_max3_u32 v108, v100, v101, v102\n" \
"v_max3_u32 v109, v103, v104, v105\n" \
"v_max3_u32 v108, v108, v106, v107\n" \
"v_max_u32 v108, v108, v109\n" \
"v_cmp_eq_u32 vcc, -1, v108\n" \
"s_nop 1\n" \
"s_cbranch_vccz .Lx_go_%=\n" \
"s_sub_u32 s49, s49, 1\n" \
"s_cbranch_scc0 .Lx_again_%=\n" \
"s_mov_b32 s49, 0\n" \
"s_branch .Lx_go_%=\n" \
".Lx_again_%=:\n" \
"s_sleep 4\n" \
"buffer_load_dwordx4 v[100:103], v20, s[64:67], s55 offen offset:0 sc1\n" \
"buffer_load_dwordx4 v[104:107], v20, s[64:67], s55 offen offset:1024 sc1\n" \
"s_waitcnt vmcnt(0)\n" \
"s_branch .Lx_chk_%=\n" \
".Lx_go_%=:\n" \
"v_perm_b32 v108, v102, v100, s46\n" \
"v_perm_b32 v109, v102, v100, s48\n" \
"v_perm_b32 v110, v103, v101, s46\n" \
"v_perm_b32 v111, v103, v101, s48\n" \
"v_perm_b32 v112, v106, v104, s46\n" \
"v_perm_b32 v113, v106, v104, s48\n" \
"v_perm_b32 v114, v107, v105, s46\n" \
"v_perm_b32 v115, v107, v105, s48\n" \
".Lx_wr_%=:\n" \
"ds_write2st64_b32 v3, v108, v109 offset0:0 offset1:1\n" \
"ds_write2st64_b32 v3, v110, v111 offset0:2 offset1:3\n" \
"ds_write2st64_b32 v3, v112, v113 offset0:4 offset1:5\n" \
"ds_write2st64_b32 v3, v114, v115 offset0:6 offset1:7\n" \
"s_branch .Lx_join_%=\n" \
".Lx_m0_%=:\n" \
"s_mov_b32 s59, 1\n" \
"s_lshl_b32 s43, s36, 8\n" \
"buffer_load_dword v108, v25, s[60:63], s43 offen offset:0\n" \
"buffer_load_dword v109, v25, s[60:63], s43 offen offset:32\n" \
"buffer_load_dword v110, v25, s[60:63], s43 offen offset:64\n" \
"buffer_load_dword v111, v25, s[60:63], s43 offen offset:96\n" \
"buffer_load_dword v112, v25, s[60:63], s43 offen offset:128\n" \
"buffer_load_dword v113, v25, s[60:63], s43 offen offset:160\n" \
"buffer_load_dword v114, v25, s[60:63], s43 offen offset:192\n" \
"buffer_load_dword v115, v25, s[60:63], s43 offen offset:224\n" \
"s_branch .Lx_a1_%=\n" \
".Lx_end_%=:\n" \
"s_mov_b32 %[redo], 0\n" \
"s_branch .Lx_out_%=\n" \
".Lx_redo_%=:\n" \
"s_waitcnt vmcnt(0) lgkmcnt(0)\n" \
"s_barrier\n" \
"s_and_b32 s98, s98, 0x7f\n" \
"s_or_b32 %[redo], s98, 0x80\n" \
".Lx_out_%=:\n"

#define OLOOP_ASM \
"v_add_u32 v145, 4096, v144\n" \
"v_add_u32 v146, 8192, v144\n" \
"v_add_u32 v147, 12288, v144\n" \
"s_mov_b32 s36, 0\n" \
"s_mov_b32 s38, 0\n" \
"s_mov_b32 s44, 0x00ff00ff\n" \
"s_mov_b32 s45, 0x00ff00ff\n" \
"s_mov_b32 s41, 0x40000\n" \
".Lo_poll0_%=:\n" \
"s_mov_b32 s37, 0\n" \
"buffer_load_dwordx4 v[136:139], v152, %[ws], %[fo] offen sc1\n" \
"s_waitcnt vmcnt(0)\n" \
"v_min3_i32 v136, v136, v137, v138\n" \
"v_min_i32 v136, v136, v139\n" \
"s_add_u32 s42, s36, 0\n" \
"v_cmp_ge_i32 vcc, s42, v136\n" \
"s_and_b64 vcc, vcc, s[44:45]\n" \
"s_cbranch_scc1 .Lo_idle0_%=\n" \
".Lo_get0_%=:\n" \
"s_add_u32 s39, s36, 0\n" \
"s_lshl_b32 s39, s39, 18\n" \
"s_add_u32 s39, s39, 41943040\n" \
"buffer_load_dwordx4 v[0:3], v144, %[ws], s39 offen offset:0 sc1\n" \
"buffer_load_dwordx4 v[4:7], v144, %[ws], s39 offen offset:1024 sc1\n" \
"buffer_load_dwordx4 v[8:11], v144, %[ws], s39 offen offset:2048 sc1\n" \
"buffer_load_dwordx4 v[12:15], v144, %[ws], s39 offen offset:3072 sc1\n" \
"buffer_load_dwordx4 v[16:19], v145, %[ws], s39 offen offset:0 sc1\n" \
"buffer_load_dwordx4 v[20:23], v145, %[ws], s39 offen offset:1024 sc1\n" \
"buffer_load_dwordx4 v[24:27], v145, %[ws], s39 offen offset:2048 sc1\n" \
"buffer_load_dwordx4 v[28:31], v145, %[ws], s39 offen offset:3072 sc1\n" \
"buffer_load_dwordx4 v[32:35], v146, %[ws], s39 offen offset:0 sc1\n" \
"buffer_load_dwordx4 v[36:39], v146, %[ws], s39 offen offset:1024 sc1\n" \
"buffer_load_dwordx4 v[40:43], v146, %[ws], s39 offen offset:2048 sc1\n" \
"buffer_load_dwordx4 v[44:47], v146, %[ws], s39 offen offset:3072 sc1\n" \
"buffer_load_dwordx4 v[48:51], v147, %[ws], s39 offen offset:0 sc1\n" \
"buffer_load_dwordx4 v[52:55], v147, %[ws], s39 offen offset:1024 sc1\n" \
"buffer_load_dwordx4 v[56:59], v147, %[ws], s39 offen offset:2048 sc1\n" \
"buffer_load_dwordx4 v[60:63], v147, %[ws], s39 offen offset:3072 sc1\n" \
".Lo_s10_%=:\n" \
"s_add_u32 s42, s36, 1\n" \
"v_cmp_ge_i32 vcc, s42, v136\n" \
"s_andn2_b64 vcc, vcc, s[44:45]\n" \
"s_cbranch_scc1 .Lo_n10_%=\n" \
"s_add_u32 s39, s36, 1\n" \
"s_lshl_b32 s39, s39, 18\n" \
"s_add_u32 s39, s39, 41943040\n" \
"s_add_u32 s42, s36, 2\n" \
"v_cmp_ge_i32 vcc, s42, v136\n" \
"s_and_b64 vcc, vcc, s[44:45]\n" \
"s_cbranch_scc1 .Lo_c10_%=\n" \
"s_add_u32 s40, s36, 2\n" \
"s_lshl_b32 s40, s40, 18\n" \
"s_add_u32 s40, s40, 41943040\n" \
"buffer_load_dwordx4 v[136:139], v152, %[ws], %[fo] offen sc1\n" \
"v_mov_b64 v[128:129], 0\n" \
"v_mov_b64 v[130:131], 0\n" \
"v_mov_b64 v[132:133], 0\n" \
"v_mov_b64 v[134:135], 0\n" \
"s_nop 0\n" \
"s_waitcnt vmcnt(15)\n" \
"v_smfmac_f32_16x16x64_bf16 v[128:131], v[0:3], a[0:7], v150\n" \
"v_smfmac_f32_16x16x64_bf16 v[132:135], v[0:3], a[8:15], v150\n" \
"buffer_load_dwordx4 v[64:67], v144, %[ws], s39 offen offset:0 sc1\n" \
"buffer_load_dwordx4 v[68:71], v144, %[ws], s39 offen offset:1024 sc1\n" \
"s_waitcnt vmcnt(16)\n" \
"v_smfmac_f32_16x16x64_bf16 v[128:131], v[4:7], a[16:23], v150\n" \
"v_smfmac_f32_16x16x64_bf16 v[132:135], v[4:7], a[24:31], v150\n" \
"buffer_load_dwordx4 v[72:75], v144, %[ws], s39 offen offset:2048 sc1\n" \
"buffer_load_dwordx4 v[76:79], v144, %[ws], s39 offen offset:3072 sc1\n" \
"s_waitcnt vmcnt(17)\n" \
"v_smfmac_f32_16x16x64_bf16 v[128:131], v[8:11], a[32:39], v150\n" \
"v_smfmac_f32_16x16x64_bf16 v[132:135], v[8:11], a[40:47], v150\n" \
"buffer_load_dwordx4 v[80:83], v145, %[ws], s39 offen offset:0 sc1\n" \
"buffer_load_dwordx4 v[84:87], v145, %[ws], s39 offen offset:1024 sc1\n" \
"s_cmp_eq_u32 s37, 0\n" \
"s_cbranch_scc1 .Lo_d10f_%=\n" \
"s_waitcnt lgkmcnt(0)\n" \
"s_barrier\n" \
"ds_read_b32 v140, v154 offset:0\n" \
"ds_read_b32 v141, v154 offset:1024\n" \
"ds_read_b32 v142, v154 offset:2048\n" \
"ds_read_b32 v143, v154 offset:3072\n" \
"v_xor_b32 v154, 4096, v154\n" \
"s_waitcnt lgkmcnt(0)\n" \
"v_add_f32 v140, v140, v141\n" \
"v_add_f32 v142, v142, v143\n" \
"v_add_f32 v140, v140, v142\n" \
"v_cvt_pk_bf16_f32 v140, 0, v140\n" \
"buffer_store_dword v140, v153, %[out], s38 offen sc1\n" \
"s_add_u32 s38, s38, 131072\n" \
".Lo_d10f_%=:\n" \
"s_waitcnt vmcnt(18)\n" \
"v_smfmac_f32_16x16x64_bf16 v[128:131], v[12:15], a[48:55], v150\n" \
"v_smfmac_f32_16x16x64_bf16 v[132:135], v[12:15], a[56:63], v150\n" \
"buffer_load_dwordx4 v[88:91], v145, %[ws], s39 offen offset:2048 sc1\n" \
"buffer_load_dwordx4 v[92:95], v145, %[ws], s39 offen offset:3072 sc1\n" \
"s_waitcnt vmcnt(19)\n" \
"v_smfmac_f32_16x16x64_bf16 v[128:131], v[16:19], a[64:71], v150\n" \
"v_smfmac_f32_16x16x64_bf16 v[132:135], v[16:19], a[72:79], v150\n" \
"buffer_load_dwordx4 v[96:99], v146, %[ws], s39 offen offset:0 sc1\n" \
"buffer_load_dwordx4 v[100:103], v146, %[ws], s39 offen offset:1024 sc1\n" \
"s_waitcnt vmcnt(20)\n" \
"v_smfmac_f32_16x16x64_bf16 v[128:131], v[20:23], a[80:87], v150\n" \
"v_smfmac_f32_16x16x64_bf16 v[132:135], v[20:23], a[88:95], v150\n" \
"buffer_load_dwordx4 v[104:107], v146, %[ws], s39 offen offset:2048 sc1\n" \
"buffer_load_dwordx4 v[108:111], v146, %[ws], s39 offen offset:3072 sc1\n" \
"s_waitcnt vmcnt(21)\n" \
"v_smfmac_f32_16x16x64_bf16 v[128:131], v[24:27], a[96:103], v150\n" \
"v_smfmac_f32_16x16x64_bf16 v[132:135], v[24:27], a[104:111], v150\n" \
"buffer_load_dwordx4 v[112:115], v147, %[ws], s39 offen offset:0 sc1\n" \
"buffer_load_dwordx4 v[116:119], v147, %[ws], s39 offen offset:1024 sc1\n" \
"s_waitcnt vmcnt(22)\n" \
"v_smfmac_f32_16x16x64_bf16 v[128:131], v[28:31], a[112:119], v150\n" \
"v_smfmac_f32_16x16x64_bf16 v[132:135], v[28:31], a[120:127], v150\n" \
"buffer_load_dwordx4 v[120:123], v147, %[ws], s39 offen offset:2048 sc1\n" \
"buffer_load_dwordx4 v[124:127], v147, %[ws], s39 offen offset:3072 sc1\n" \
"s_waitcnt vmcnt(23)\n" \
"v_smfmac_f32_16x16x64_bf16 v[128:131], v[32:35], a[128:135], v150\n" \
"v_smfmac_f32_16x16x64_bf16 v[132:135], v[32:35], a[136:143], v150\n" \
"buffer_load_dwordx4 v[0:3], v144, %[ws], s40 offen offset:0 sc1\n" \
"buffer_load_dwordx4 v[4:7], v144, %[ws], s40 offen offset:1024 sc1\n" \
"s_waitcnt vmcnt(24)\n" \
"v_smfmac_f32_16x16x64_bf16 v[128:131], v[36:39], a[144:151], v150\n" \
"v_smfmac_f32_16x16x64_bf16 v[132:135], v[36:39], a[152:159], v150\n" \
"buffer_load_dwordx4 v[8:11], v144, %[ws], s40 offen offset:2048 sc1\n" \
"buffer_load_dwordx4 v[12:15], v144, %[ws], s40 offen offset:3072 sc1\n" \
"s_waitcnt vmcnt(25)\n" \
"v_smfmac_f32_16x16x64_bf16 v[128:131], v[40:43], a[160:167], v150\n" \
"v_smfmac_f32_16x16x64_bf16 v[132:135], v[40:43], a[168:175], v150\n" \
"buffer_load_dwordx4 v[16:19], v145, %[ws], s40 offen offset:0 sc1\n" \
"buffer_load_dwordx4 v[20:23], v145, %[ws], s40 offen offset:1024 sc1\n" \
"s_waitcnt vmcnt(26)\n" \
"v_smfmac_f32_16x16x64_bf16 v[128:131], v[44:47], a[176:183], v150\n" \
"v_smfmac_f32_16x16x64_bf16 v[132:135], v[44:47], a[184:191], v150\n" \
"buffer_load_dwordx4 v[24:27], v145, %[ws], s40 offen offset:2048 sc1\n" \
"buffer_load_dwordx4 v[28:31], v145, %[ws], s40 offen offset:3072 sc1\n" \
"s_waitcnt vmcnt(27)\n" \
"v_smfmac_f32_16x16x64_bf16 v[128:131], v[48:51], a[192:199], v150\n" \
"v_smfmac_f32_16x16x64_bf16 v[132:135], v[48:51], a[200:207], v150\n" \
"buffer_load_dwordx4 v[32:35], v146, %[ws], s40 offen offset:0 sc1\n" \
"buffer_load_dwordx4 v[36:39], v146, %[ws], s40 offen offset:1024 sc1\n" \
"s_waitcnt vmcnt(28)\n" \
"v_smfmac_f32_16x16x64_bf16 v[128:131], v[52:55], a[208:215], v150\n" \
"v_smfmac_f32_16x16x64_bf16 v[132:135], v[52:55], a[216:223], v150\n" \
"buffer_load_dwordx4 v[40:43], v146, %[ws], s40 offen offset:2048 sc1\n" \
"buffer_load_dwordx4 v[44:47], v146, %[ws], s40 offen offset:3072 sc1\n" \
"s_waitcnt vmcnt(29)\n" \
"v_smfmac_f32_16x16x64_bf16 v[128:131], v[56:59], a[224:231], v150\n" \
"v_smfmac_f32_16x16x64_bf16 v[132:135], v[56:59], a[232:239], v150\n" \
"buffer_load_dwordx4 v[48:51], v147, %[ws], s40 offen offset:0 sc1\n" \
"buffer_load_dwordx4 v[52:55], v147, %[ws], s40 offen offset:1024 sc1\n" \
"s_waitcnt vmcnt(30)\n" \
"v_smfmac_f32_16x16x64_bf16 v[128:131], v[60:63], a[240:247], v150\n" \
"v_smfmac_f32_16x16x64_bf16 v[132:135], v[60:63], a[248:255], v150\n" \
"buffer_load_dwordx4 v[56:59], v147, %[ws], s40 offen offset:2048 sc1\n" \
"buffer_load_dwordx4 v[60:63], v147, %[ws], s40 offen offset:3072 sc1\n" \
"s_nop 7\n" \
"s_nop 1\n" \
"v_permlane32_swap_b32 v128, v132\n" \
"v_permlane32_swap_b32 v129, v133\n" \
"v_permlane32_swap_b32 v130, v134\n" \
"v_permlane32_swap_b32 v131, v135\n" \
"s_nop 0\n" \
"v_pk_add_f32 v[128:129], v[128:129], v[132:133]\n" \
"v_pk_add_f32 v[130:131], v[130:131], v[134:135]\n" \
"s_nop 0\n" \
"ds_write_b128 v155, v[128:131]\n" \
"v_xor_b32 v155, 4096, v155\n" \
"s_add_u32 s36, s36, 1\n" \
"s_not_b64 s[44:45], s[44:45]\n" \
"s_mov_b32 s37, 1\n" \
"s_waitcnt vmcnt(32)\n" \
"v_min3_i32 v136, v136, v137, v138\n" \
"v_min_i32 v136, v136, v139\n" \
"s_branch .Lo_s21_%=\n" \
".Lo_c10_%=:\n" \
"buffer_load_dwordx4 v[136:139], v152, %[ws], %[fo] offen sc1\n" \
"v_mov_b64 v[128:129], 0\n" \
"v_mov_b64 v[130:131], 0\n" \
"v_mov_b64 v[132:133], 0\n" \
"v_mov_b64 v[134:135], 0\n" \
"s_nop 0\n" \
"s_waitcnt vmcnt(15)\n" \
"v_smfmac_f32_16x16x64_bf16 v[128:131], v[0:3], a[0:7], v150\n" \
"v_smfmac_f32_16x16x64_bf16 v[132:135], v[0:3], a[8:15], v150\n" \
"buffer_load_dwordx4 v[64:67], v144, %[ws], s39 offen offset:0 sc1\n" \
"s_waitcnt vmcnt(15)\n" \
"v_smfmac_f32_16x16x64_bf16 v[128:131], v[4:7], a[16:23], v150\n" \
"v_smfmac_f32_16x16x64_bf16 v[132:135], v[4:7], a[24:31], v150\n" \
"buffer_load_dwordx4 v[68:71], v144, %[ws], s39 offen offset:1024 sc1\n" \
"s_waitcnt vmcnt(15)\n" \
"v_smfmac_f32_16x16x64_bf16 v[128:131], v[8:11], a[32:39], v150\n" \
"v_smfmac_f32_16x16x64_bf16 v[132:135], v[8:11], a[40:47], v150\n" \
"buffer_load_dwordx4 v[72:75], v144, %[ws], s39 offen offset:2048 sc1\n" \
"s_cmp_eq_u32 s37, 0\n" \
"s_cbranch_scc1 .Lo_c10f_%=\n" \
"s_waitcnt lgkmcnt(0)\n" \
"s_barrier\n" \
"ds_read_b32 v140, v154 offset:0\n" \
"ds_read_b32 v141, v154 offset:1024\n" \
"ds_read_b32 v142, v154 offset:2048\n" \
"ds_read_b32 v143, v154 offset:3072\n" \
"v_xor_b32 v154, 4096, v154\n" \
"s_waitcnt lgkmcnt(0)\n" \
"v_add_f32 v140, v140, v141\n" \
"v_add_f32 v142, v142, v143\n" \
"v_add_f32 v140, v140, v142\n" \
"v_cvt_pk_bf16_f32 v140, 0, v140\n" \
"buffer_store_dword v140, v153, %[out], s38 offen sc1\n" \
"s_add_u32 s38, s38, 131072\n" \
".Lo_c10f_%=:\n" \
"s_waitcnt vmcnt(15)\n" \
"v_smfmac_f32_16x16x64_bf16 v[128:131], v[12:15], a[48:55], v150\n" \
"v_smfmac_f32_16x16x64_bf16 v[132:135], v[12:15], a[56:63], v150\n" \
"buffer_load_dwordx4 v[76:79], v144, %[ws], s39 offen offset:3072 sc1\n" \
"s_waitcnt vmcnt(15)\n" \
"v_smfmac_f32_16x16x64_bf16 v[128:131], v[16:19], a[64:71], v150\n" \
"v_smfmac_f32_16x16x64_bf16 v[132:135], v[16:19], a[72:79], v150\n" \
"buffer_load_dwordx4 v[80:83], v145, %[ws], s39 offen offset:0 sc1\n" \
"s_waitcnt vmcnt(15)\n" \
"v_smfmac_f32_16x16x64_bf16 v[128:131], v[20:23], a[80:87], v150\n" \
"v_smfmac_f32_16x16x64_bf16 v[132:135], v[20:23], a[88:95], v150\n" \
"buffer_load_dwordx4 v[84:87], v145, %[ws], s39 offen offset:1024 sc1\n" \
"s_waitcnt vmcnt(15)\n" \
"v_smfmac_f32_16x16x64_bf16 v[128:131], v[24:27], a[96:103], v150\n" \
"v_smfmac_f32_16x16x64_bf16 v[132:135], v[24:27], a[104:111], v150\n" \
"buffer_load_dwordx4 v[88:91], v145, %[ws], s39 offen offset:2048 sc1\n" \
"s_waitcnt vmcnt(15)\n" \
"v_smfmac_f32_16x16x64_bf16 v[128:131], v[28:31], a[112:119], v150\n" \
"v_smfmac_f32_16x16x64_bf16 v[132:135], v[28:31], a[120:127], v150\n" \
"buffer_load_dwordx4 v[92:95], v145, %[ws], s39 offen offset:3072 sc1\n" \
"s_waitcnt vmcnt(15)\n" \
"v_smfmac_f32_16x16x64_bf16 v[128:131], v[32:35], a[128:135], v150\n" \
"v_smfmac_f32_16x16x64_bf16 v[132:135], v[32:35], a[136:143], v150\n" \
"buffer_load_dwordx4 v[96:99], v146, %[ws], s39 offen offset:0 sc1\n" \
"s_waitcnt vmcnt(15)\n" \
"v_smfmac_f32_16x16x64_bf16 v[128:131], v[36:39], a[144:151], v150\n" \
"v_smfmac_f32_16x16x64_bf16 v[132:135], v[36:39], a[152:159], v150\n" \
"buffer_load_dwordx4 v[100:103], v146, %[ws], s39 offen offset:1024 sc1\n" \
"s_waitcnt vmcnt(15)\n" \
"v_smfmac_f32_16x16x64_bf16 v[128:131], v[40:43], a[160:167], v150\n" \
"v_smfmac_f32_16x16x64_bf16 v[132:135], v[40:43], a[168:175], v150\n" \
"buffer_load_dwordx4 v[104:107], v146, %[ws], s39 offen offset:2048 sc1\n" \
"s_waitcnt vmcnt(15)\n" \
"v_smfmac_f32_16x16x64_bf16 v[128:131], v[44:47], a[176:183], v150\n" \
"v_smfmac_f32_16x16x64_bf16 v[132:135], v[44:47], a[184:191], v150\n" \
"buffer_load_dwordx4 v[108:111], v146, %[ws], s39 offen offset:3072 sc1\n" \
"s_waitcnt vmcnt(15)\n" \
"v_smfmac_f32_16x16x64_bf16 v[128:131], v[48:51], a[192:199], v150\n" \
"v_smfmac_f32_16x16x64_bf16 v[132:135], v[48:51], a[200:207], v150\n" \
"buffer_load_dwordx4 v[112:115], v147, %[ws], s39 offen offset:0 sc1\n" \
"s_waitcnt vmcnt(15)\n" \
"v_smfmac_f32_16x16x64_bf16 v[128:131], v[52:55], a[208:215], v150\n" \
"v_smfmac_f32_16x16x64_bf16 v[132:135], v[52:55], a[216:223], v150\n" \
"buffer_load_dwordx4 v[116:119], v147, %[ws], s39 offen offset:1024 sc1\n" \
"s_waitcnt vmcnt(15)\n" \
"v_smfmac_f32_16x16x64_bf16 v[128:131], v[56:59], a[224:231], v150\n" \
"v_smfmac_f32_16x16x64_bf16 v[132:135], v[56:59], a[232:239], v150\n" \
"buffer_load_dwordx4 v[120:123], v147, %[ws], s39 offen offset:2048 sc1\n" \
"s_waitcnt vmcnt(15)\n" \
"v_smfmac_f32_16x16x64_bf16 v[128:131], v[60:63], a[240:247], v150\n" \
"v_smfmac_f32_16x16x64_bf16 v[132:135], v[60:63], a[248:255], v150\n" \
"buffer_load_dwordx4 v[124:127], v147, %[ws], s39 offen offset:3072 sc1\n" \
"s_nop 7\n" \
"s_nop 1\n" \
"v_permlane32_swap_b32 v128, v132\n" \
"v_permlane32_swap_b32 v129, v133\n" \
"v_permlane32_swap_b32 v130, v134\n" \
"v_permlane32_swap_b32 v131, v135\n" \
"s_nop 0\n" \
"v_pk_add_f32 v[128:129], v[128:129], v[132:133]\n" \
"v_pk_add_f32 v[130:131], v[130:131], v[134:135]\n" \
"s_nop 0\n" \
"ds_write_b128 v155, v[128:131]\n" \
"v_xor_b32 v155, 4096, v155\n" \
"s_add_u32 s36, s36, 1\n" \
"s_not_b64 s[44:45], s[44:45]\n" \
"s_mov_b32 s37, 1\n" \
"s_waitcnt vmcnt(16)\n" \
"v_min3_i32 v136, v136, v137, v138\n" \
"v_min_i32 v136, v136, v139\n" \
"s_branch .Lo_s11_%=\n" \
".Lo_n10_%=:\n" \
"buffer_load_dwordx4 v[136:139], v152, %[ws], %[fo] offen sc1\n" \
"v_mov_b64 v[128:129], 0\n" \
"v_mov_b64 v[130:131], 0\n" \
"v_mov_b64 v[132:133], 0\n" \
"v_mov_b64 v[134:135], 0\n" \
"s_nop 0\n" \
"s_waitcnt vmcnt(15)\n" \
"v_smfmac_f32_16x16x64_bf16 v[128:131], v[0:3], a[0:7], v150\n" \
"v_smfmac_f32_16x16x64_bf16 v[132:135], v[0:3], a[8:15], v150\n" \
"s_waitcnt vmcnt(14)\n" \
"v_smfmac_f32_16x16x64_bf16 v[128:131], v[4:7], a[16:23], v150\n" \
"v_smfmac_f32_16x16x64_bf16 v[132:135], v[4:7], a[24:31], v150\n" \
"s_waitcnt vmcnt(13)\n" \
"v_smfmac_f32_16x16x64_bf16 v[128:131], v[8:11], a[32:39], v150\n" \
"v_smfmac_f32_16x16x64_bf16 v[132:135], v[8:11], a[40:47], v150\n" \
"s_cmp_eq_u32 s37, 0\n" \
"s_cbranch_scc1 .Lo_n10f_%=\n" \
"s_waitcnt lgkmcnt(0)\n" \
"s_barrier\n" \
"ds_read_b32 v140, v154 offset:0\n" \
"ds_read_b32 v141, v154 offset:1024\n" \
"ds_read_b32 v142, v154 offset:2048\n" \
"ds_read_b32 v143, v154 offset:3072\n" \
"v_xor_b32 v154, 4096, v154\n" \
"s_waitcnt lgkmcnt(0)\n" \
"v_add_f32 v140, v140, v141\n" \
"v_add_f32 v142, v142, v143\n" \
"v_add_f32 v140, v140, v142\n" \
"v_cvt_pk_bf16_f32 v140, 0, v140\n" \
"buffer_store_dword v140, v153, %[out], s38 offen sc1\n" \
"s_add_u32 s38, s38, 131072\n" \
".Lo_n10f_%=:\n" \
"s_waitcnt vmcnt(12)\n" \
"v_smfmac_f32_16x16x64_bf16 v[128:131], v[12:15], a[48:55], v150\n" \
"v_smfmac_f32_16x16x64_bf16 v[132:135], v[12:15], a[56:63], v150\n" \
"s_waitcnt vmcnt(11)\n" \
"v_smfmac_f32_16x16x64_bf16 v[128:131], v[16:19], a[64:71], v150\n" \
"v_smfmac_f32_16x16x64_bf16 v[132:135], v[16:19], a[72:79], v150\n" \
"s_waitcnt vmcnt(10)\n" \
"v_smfmac_f32_16x16x64_bf16 v[128:131], v[20:23], a[80:87], v150\n" \
"v_smfmac_f32_16x16x64_bf16 v[132:135], v[20:23], a[88:95], v150\n" \
"s_waitcnt vmcnt(9)\n" \
"v_smfmac_f32_16x16x64_bf16 v[128:131], v[24:27], a[96:103], v150\n" \
"v_smfmac_f32_16x16x64_bf16 v[132:135], v[24:27], a[104:111], v150\n" \
"s_waitcnt vmcnt(8)\n" \
"v_smfmac_f32_16x16x64_bf16 v[128:131], v[28:31], a[112:119], v150\n" \
"v_smfmac_f32_16x16x64_bf16 v[132:135], v[28:31], a[120:127], v150\n" \
"s_waitcnt vmcnt(7)\n" \
"v_smfmac_f32_16x16x64_bf16 v[128:131], v[32:35], a[128:135], v150\n" \
"v_smfmac_f32_16x16x64_bf16 v[132:135], v[32:35], a[136:143], v150\n" \
"s_waitcnt vmcnt(6)\n" \
"v_smfmac_f32_16x16x64_bf16 v[128:131], v[36:39], a[144:151], v150\n" \
"v_smfmac_f32_16x16x64_bf16 v[132:135], v[36:39], a[152:159], v150\n" \
"s_waitcnt vmcnt(5)\n" \
"v_smfmac_f32_16x16x64_bf16 v[128:131], v[40:43], a[160:167], v150\n" \
"v_smfmac_f32_16x16x64_bf16 v[132:135], v[40:43], a[168:175], v150\n" \
"s_waitcnt vmcnt(4)\n" \
"v_smfmac_f32_16x16x64_bf16 v[128:131], v[44:47], a[176:183], v150\n" \
"v_smfmac_f32_16x16x64_bf16 v[132:135], v[44:47], a[184:191], v150\n" \
"s_waitcnt vmcnt(3)\n" \
"v_smfmac_f32_16x16x64_bf16 v[128:131], v[48:51], a[192:199], v150\n" \
"v_smfmac_f32_16x16x64_bf16 v[132:135], v[48:51], a[200:207], v150\n" \
"s_waitcnt vmcnt(2)\n" \
"v_smfmac_f32_16x16x64_bf16 v[128:131], v[52:55], a[208:215], v150\n" \
"v_smfmac_f32_16x16x64_bf16 v[132:135], v[52:55], a[216:223], v150\n" \
"s_waitcnt vmcnt(1)\n" \
"v_smfmac_f32_16x16x64_bf16 v[128:131], v[56:59], a[224:231], v150\n" \
"v_smfmac_f32_16x16x64_bf16 v[132:135], v[56:59], a[232:239], v150\n" \
"s_waitcnt vmcnt(0)\n" \
"v_smfmac_f32_16x16x64_bf16 v[128:131], v[60:63], a[240:247], v150\n" \
"v_smfmac_f32_16x16x64_bf16 v[132:135], v[60:63], a[248:255], v150\n" \
"s_nop 7\n" \
"s_nop 1\n" \
"v_permlane32_swap_b32 v128, v132\n" \
"v_permlane32_swap_b32 v129, v133\n" \
"v_permlane32_swap_b32 v130, v134\n" \
"v_permlane32_swap_b32 v131, v135\n" \
"s_nop 0\n" \
"v_pk_add_f32 v[128:129], v[128:129], v[132:133]\n" \
"v_pk_add_f32 v[130:131], v[130:131], v[134:135]\n" \
"s_nop 0\n" \
"ds_write_b128 v155, v[128:131]\n" \
"v_xor_b32 v155, 4096, v155\n" \
"s_add_u32 s36, s36, 1\n" \
"s_not_b64 s[44:45], s[44:45]\n" \
"s_cmp_eq_u32 s37, 0\n" \
"s_cbranch_scc1 .Lo_w00_%=\n" \
"s_waitcnt vmcnt(1)\n" \
"s_branch .Lo_w10_%=\n" \
".Lo_w00_%=:\n" \
"s_waitcnt vmcnt(0)\n" \
".Lo_w10_%=:\n" \
"v_min3_i32 v136, v136, v137, v138\n" \
"v_min_i32 v136, v136, v139\n" \
"s_mov_b32 s37, 1\n" \
"s_add_u32 s42, s36, 0\n" \
"v_cmp_ge_i32 vcc, s42, v136\n" \
"s_and_b64 vcc, vcc, s[44:45]\n" \
"s_cbranch_scc1 .Lo_dry0_%=\n" \
"s_branch .Lo_get0_%=\n" \
".Lo_dry0_%=:\n" \
"s_waitcnt lgkmcnt(0)\n" \
"s_barrier\n" \
"ds_read_b32 v140, v154 offset:0\n" \
"ds_read_b32 v141, v154 offset:1024\n" \
"ds_read_b32 v142, v154 offset:2048\n" \
"ds_read_b32 v143, v154 offset:3072\n" \
"v_xor_b32 v154, 4096, v154\n" \
"s_waitcnt lgkmcnt(0)\n" \
"v_add_f32 v140, v140, v141\n" \
"v_add_f32 v142, v142, v143\n" \
"v_add_f32 v140, v140, v142\n" \
"v_cvt_pk_bf16_f32 v140, 0, v140\n" \
"buffer_store_dword v140, v153, %[out], s38 offen sc1\n" \
"s_add_u32 s38, s38, 131072\n" \
"s_cmp_eq_u32 s36, 64\n" \
"s_cbranch_scc1 .Lo_end_%=\n" \
"s_branch .Lo_poll0_%=\n" \
".Lo_idle0_%=:\n" \
"s_sub_u32 s41, s41, 1\n" \
"s_cbranch_scc1 .Lo_end_%=\n" \
"s_cmp_ge_u32 s36, 62\n" \
"s_cbranch_scc1 .Lo_poll0_%=\n" \
"s_sleep 8\n" \
"s_branch .Lo_poll0_%=\n" \
".Lo_s20_%=:\n" \
"s_add_u32 s42, s36, 2\n" \
"v_cmp_ge_i32 vcc, s42, v136\n" \
"s_and_b64 vcc, vcc, s[44:45]\n" \
"s_cbranch_scc1 .Lo_n20_%=\n" \
"s_add_u32 s40, s36, 2\n" \
"s_lshl_b32 s40, s40, 18\n" \
"s_add_u32 s40, s40, 41943040\n" \
"buffer_load_dwordx4 v[136:139], v152, %[ws], %[fo] offen sc1\n" \
"v_mov_b64 v[128:129], 0\n" \
"v_mov_b64 v[130:131], 0\n" \
"v_mov_b64 v[132:133], 0\n" \
"v_mov_b64 v[134:135], 0\n" \
"s_nop 0\n" \
"s_waitcnt vmcnt(31)\n" \
"v_smfmac_f32_16x16x64_bf16 v[128:131], v[0:3], a[0:7], v150\n" \
"v_smfmac_f32_16x16x64_bf16 v[132:135], v[0:3], a[8:15], v150\n" \
"buffer_load_dwordx4 v[0:3], v144, %[ws], s40 offen offset:0 sc1\n" \
"s_waitcnt vmcnt(31)\n" \
"v_smfmac_f32_16x16x64_bf16 v[128:131], v[4:7], a[16:23], v150\n" \
"v_smfmac_f32_16x16x64_bf16 v[132:135], v[4:7], a[24:31], v150\n" \
"buffer_load_dwordx4 v[4:7], v144, %[ws], s40 offen offset:1024 sc1\n" \
"s_waitcnt vmcnt(31)\n" \
"v_smfmac_f32_16x16x64_bf16 v[128:131], v[8:11], a[32:39], v150\n" \
"v_smfmac_f32_16x16x64_bf16 v[132:135], v[8:11], a[40:47], v150\n" \
"buffer_load_dwordx4 v[8:11], v144, %[ws], s40 offen offset:2048 sc1\n" \
"s_waitcnt lgkmcnt(0)\n" \
"s_barrier\n" \
"ds_read_b32 v140, v154 offset:0\n" \
"ds_read_b32 v141, v154 offset:1024\n" \
"ds_read_b32 v142, v154 offset:2048\n" \
"ds_read_b32 v143, v154 offset:3072\n" \
"v_xor_b32 v154, 4096, v154\n" \
"s_waitcnt lgkmcnt(0)\n" \
"v_add_f32 v140, v140, v141\n" \
"v_add_f32 v142, v142, v143\n" \
"v_add_f32 v140, v140, v142\n" \
"v_cvt_pk_bf16_f32 v140, 0, v140\n" \
"buffer_store_dword v140, v153, %[out], s38 offen sc1\n" \
"s_add_u32 s38, s38, 131072\n" \
"s_waitcnt vmcnt(31)\n" \
"v_smfmac_f32_16x16x64_bf16 v[128:131], v[12:15], a[48:55], v150\n" \
"v_smfmac_f32_16x16x64_bf16 v[132:135], v[12:15], a[56:63], v150\n" \
"buffer_load_dwordx4 v[12:15], v144, %[ws], s40 offen offset:3072 sc1\n" \
"s_waitcnt vmcnt(31)\n" \
"v_smfmac_f32_16x16x64_bf16 v[128:131], v[16:19], a[64:71], v150\n" \
"v_smfmac_f32_16x16x64_bf16 v[132:135], v[16:19], a[72:79], v150\n" \
"buffer_load_dwordx4 v[16:19], v145, %[ws], s40 offen offset:0 sc1\n" \
"s_waitcnt vmcnt(31)\n" \
"v_smfmac_f32_16x16x64_bf16 v[128:131], v[20:23], a[80:87], v150\n" \
"v_smfmac_f32_16x16x64_bf16 v[132:135], v[20:23], a[88:95], v150\n" \
"buffer_load_dwordx4 v[20:23], v145, %[ws], s40 offen offset:1024 sc1\n" \
"s_waitcnt vmcnt(31)\n" \
"v_smfmac_f32_16x16x64_bf16 v[128:131], v[24:27], a[96:103], v150\n" \
"v_smfmac_f32_16x16x64_bf16 v[132:135], v[24:27], a[104:111], v150\n" \
"buffer_load_dwordx4 v[24:27], v145, %[ws], s40 offen offset:2048 sc1\n" \
"s_waitcnt vmcnt(31)\n" \
"v_smfmac_f32_16x16x64_bf16 v[128:131], v[28:31], a[112:119], v150\n" \
"v_smfmac_f32_16x16x64_bf16 v[132:135], v[28:31], a[120:127], v150\n" \
"buffer_load_dwordx4 v[28:31], v145, %[ws], s40 offen offset:3072 sc1\n" \
"s_waitcnt vmcnt(31)\n" \
"v_smfmac_f32_16x16x64_bf16 v[128:131], v[32:35], a[128:135], v150\n" \
"v_smfmac_f32_16x16x64_bf16 v[132:135], v[32:35], a[136:143], v150\n" \
"buffer_load_dwordx4 v[32:35], v146, %[ws], s40 offen offset:0 sc1\n" \
"s_waitcnt vmcnt(31)\n" \
"v_smfmac_f32_16x16x64_bf16 v[128:131], v[36:39], a[144:151], v150\n" \
"v_smfmac_f32_16x16x64_bf16 v[132:135], v[36:39], a[152:159], v150\n" \
"buffer_load_dwordx4 v[36:39], v146, %[ws], s40 offen offset:1024 sc1\n" \
"s_waitcnt vmcnt(31)\n" \
"v_smfmac_f32_16x16x64_bf16 v[128:131], v[40:43], a[160:167], v150\n" \
"v_smfmac_f32_16x16x64_bf16 v[132:135], v[40:43], a[168:175], v150\n" \
"buffer_load_dwordx4 v[40:43], v146, %[ws], s40 offen offset:2048 sc1\n" \
"s_waitcnt vmcnt(31)\n" \
"v_smfmac_f32_16x16x64_bf16 v[128:131], v[44:47], a[176:183], v150\n" \
"v_smfmac_f32_16x16x64_bf16 v[132:135], v[44:47], a[184:191], v150\n" \
"buffer_load_dwordx4 v[44:47], v146, %[ws], s40 offen offset:3072 sc1\n" \
"s_waitcnt vmcnt(31)\n" \
"v_smfmac_f32_16x16x64_bf16 v[128:131], v[48:51], a[192:199], v150\n" \
"v_smfmac_f32_16x16x64_bf16 v[132:135], v[48:51], a[200:207], v150\n" \
"buffer_load_dwordx4 v[48:51], v147, %[ws], s40 offen offset:0 sc1\n" \
"s_waitcnt vmcnt(31)\n" \
"v_smfmac_f32_16x16x64_bf16 v[128:131], v[52:55], a[208:215], v150\n" \
"v_smfmac_f32_16x16x64_bf16 v[132:135], v[52:55], a[216:223], v150\n" \
"buffer_load_dwordx4 v[52:55], v147, %[ws], s40 offen offset:1024 sc1\n" \
"s_waitcnt vmcnt(31)\n" \
"v_smfmac_f32_16x16x64_bf16 v[128:131], v[56:59], a[224:231], v150\n" \
"v_smfmac_f32_16x16x64_bf16 v[132:135], v[56:59], a[232:239], v150\n" \
"buffer_load_dwordx4 v[56:59], v147, %[ws], s40 offen offset:2048 sc1\n" \
"s_waitcnt vmcnt(31)\n" \
"v_smfmac_f32_16x16x64_bf16 v[128:131], v[60:63], a[240:247], v150\n" \
"v_smfmac_f32_16x16x64_bf16 v[132:135], v[60:63], a[248:255], v150\n" \
"buffer_load_dwordx4 v[60:63], v147, %[ws], s40 offen offset:3072 sc1\n" \
"s_nop 7\n" \
"s_nop 1\n" \
"v_permlane32_swap_b32 v128, v132\n" \
"v_permlane32_swap_b32 v129, v133\n" \
"v_permlane32_swap_b32 v130, v134\n" \
"v_permlane32_swap_b32 v131, v135\n" \
"s_nop 0\n" \
"v_pk_add_f32 v[128:129], v[128:129], v[132:133]\n" \
"v_pk_add_f32 v[130:131], v[130:131], v[134:135]\n" \
"s_nop 0\n" \
"ds_write_b128 v155, v[128:131]\n" \
"v_xor_b32 v155, 4096, v155\n" \
"s_add_u32 s36, s36, 1\n" \
"s_not_b64 s[44:45], s[44:45]\n" \
"s_waitcnt vmcnt(17)\n" \
"v_min3_i32 v136, v136, v137, v138\n" \
"v_min_i32 v136, v136, v139\n" \
"s_branch .Lo_s21_%=\n" \
".Lo_n20_%=:\n" \
"buffer_load_dwordx4 v[136:139], v152, %[ws], %[fo] offen sc1\n" \
"v_mov_b64 v[128:129], 0\n" \
"v_mov_b64 v[130:131], 0\n" \
"v_mov_b64 v[132:133], 0\n" \
"v_mov_b64 v[134:135], 0\n" \
"s_nop 0\n" \
"s_waitcnt vmcnt(31)\n" \
"v_smfmac_f32_16x16x64_bf16 v[128:131], v[0:3], a[0:7], v150\n" \
"v_smfmac_f32_16x16x64_bf16 v[132:135], v[0:3], a[8:15], v150\n" \
"s_waitcnt vmcnt(30)\n" \
"v_smfmac_f32_16x16x64_bf16 v[128:131], v[4:7], a[16:23], v150\n" \
"v_smfmac_f32_16x16x64_bf16 v[132:135], v[4:7], a[24:31], v150\n" \
"s_waitcnt vmcnt(29)\n" \
"v_smfmac_f32_16x16x64_bf16 v[128:131], v[8:11], a[32:39], v150\n" \
"v_smfmac_f32_16x16x64_bf16 v[132:135], v[8:11], a[40:47], v150\n" \
"s_waitcnt lgkmcnt(0)\n" \
"s_barrier\n" \
"ds_read_b32 v140, v154 offset:0\n" \
"ds_read_b32 v141, v154 offset:1024\n" \
"ds_read_b32 v142, v154 offset:2048\n" \
"ds_read_b32 v143, v154 offset:3072\n" \
"v_xor_b32 v154, 4096, v154\n" \
"s_waitcnt lgkmcnt(0)\n" \
"v_add_f32 v140, v140, v141\n" \
"v_add_f32 v142, v142, v143\n" \
"v_add_f32 v140, v140, v142\n" \
"v_cvt_pk_bf16_f32 v140, 0, v140\n" \
"buffer_store_dword v140, v153, %[out], s38 offen sc1\n" \
"s_add_u32 s38, s38, 131072\n" \
"s_waitcnt vmcnt(28)\n" \
"v_smfmac_f32_16x16x64_bf16 v[128:131], v[12:15], a[48:55], v150\n" \
"v_smfmac_f32_16x16x64_bf16 v[132:135], v[12:15], a[56:63], v150\n" \
"s_waitcnt vmcnt(27)\n" \
"v_smfmac_f32_16x16x64_bf16 v[128:131], v[16:19], a[64:71], v150\n" \
"v_smfmac_f32_16x16x64_bf16 v[132:135], v[16:19], a[72:79], v150\n" \
"s_waitcnt vmcnt(26)\n" \
"v_smfmac_f32_16x16x64_bf16 v[128:131], v[20:23], a[80:87], v150\n" \
"v_smfmac_f32_16x16x64_bf16 v[132:135], v[20:23], a[88:95], v150\n" \
"s_waitcnt vmcnt(25)\n" \
"v_smfmac_f32_16x16x64_bf16 v[128:131], v[24:27], a[96:103], v150\n" \
"v_smfmac_f32_16x16x64_bf16 v[132:135], v[24:27], a[104:111], v150\n" \
"s_waitcnt vmcnt(24)\n" \
"v_smfmac_f32_16x16x64_bf16 v[128:131], v[28:31], a[112:119], v150\n" \
"v_smfmac_f32_16x16x64_bf16 v[132:135], v[28:31], a[120:127], v150\n" \
"s_waitcnt vmcnt(23)\n" \
"v_smfmac_f32_16x16x64_bf16 v[128:131], v[32:35], a[128:135], v150\n" \
"v_smfmac_f32_16x16x64_bf16 v[132:135], v[32:35], a[136:143], v150\n" \
"s_waitcnt vmcnt(22)\n" \
"v_smfmac_f32_16x16x64_bf16 v[128:131], v[36:39], a[144:151], v150\n" \
"v_smfmac_f32_16x16x64_bf16 v[132:135], v[36:39], a[152:159], v150\n" \
"s_waitcnt vmcnt(21)\n" \
"v_smfmac_f32_16x16x64_bf16 v[128:131], v[40:43], a[160:167], v150\n" \
"v_smfmac_f32_16x16x64_bf16 v[132:135], v[40:43], a[168:175], v150\n" \
"s_waitcnt vmcnt(20)\n" \
"v_smfmac_f32_16x16x64_bf16 v[128:131], v[44:47], a[176:183], v150\n" \
"v_smfmac_f32_16x16x64_bf16 v[132:135], v[44:47], a[184:191], v150\n" \
"s_waitcnt vmcnt(19)\n" \
"v_smfmac_f32_16x16x64_bf16 v[128:131], v[48:51], a[192:199], v150\n" \
"v_smfmac_f32_16x16x64_bf16 v[132:135], v[48:51], a[200:207], v150\n" \
"s_waitcnt vmcnt(18)\n" \
"v_smfmac_f32_16x16x64_bf16 v[128:131], v[52:55], a[208:215], v150\n" \
"v_smfmac_f32_16x16x64_bf16 v[132:135], v[52:55], a[216:223], v150\n" \
"s_waitcnt vmcnt(17)\n" \
"v_smfmac_f32_16x16x64_bf16 v[128:131], v[56:59], a[224:231], v150\n" \
"v_smfmac_f32_16x16x64_bf16 v[132:135], v[56:59], a[232:239], v150\n" \
"s_waitcnt vmcnt(16)\n" \
"v_smfmac_f32_16x16x64_bf16 v[128:131], v[60:63], a[240:247], v150\n" \
"v_smfmac_f32_16x16x64_bf16 v[132:135], v[60:63], a[248:255], v150\n" \
"s_nop 7\n" \
"s_nop 1\n" \
"v_permlane32_swap_b32 v128, v132\n" \
"v_permlane32_swap_b32 v129, v133\n" \
"v_permlane32_swap_b32 v130, v134\n" \
"v_permlane32_swap_b32 v131, v135\n" \
"s_nop 0\n" \
"v_pk_add_f32 v[128:129], v[128:129], v[132:133]\n" \
"v_pk_add_f32 v[130:131], v[130:131], v[134:135]\n" \
"s_nop 0\n" \
"ds_write_b128 v155, v[128:131]\n" \
"v_xor_b32 v155, 4096, v155\n" \
"s_add_u32 s36, s36, 1\n" \
"s_not_b64 s[44:45], s[44:45]\n" \
"s_waitcnt vmcnt(1)\n" \
"v_min3_i32 v136, v136, v137, v138\n" \
"v_min_i32 v136, v136, v139\n" \
"s_branch .Lo_s11_%=\n" \
".Lo_poll1_%=:\n" \
"s_mov_b32 s37, 0\n" \
"buffer_load_dwordx4 v[136:139], v152, %[ws], %[fo] offen sc1\n" \
"s_waitcnt vmcnt(0)\n" \
"v_min3_i32 v136, v136, v137, v138\n" \
"v_min_i32 v136, v136, v139\n" \
"s_add_u32 s42, s36, 0\n" \
"v_cmp_ge_i32 vcc, s42, v136\n" \
"s_and_b64 vcc, vcc, s[44:45]\n" \
"s_cbranch_scc1 .Lo_idle1_%=\n" \
".Lo_get1_%=:\n" \
"s_add_u32 s39, s36, 0\n" \
"s_lshl_b32 s39, s39, 18\n" \
"s_add_u32 s39, s39, 41943040\n" \
"buffer_load_dwordx4 v[64:67], v144, %[ws], s39 offen offset:0 sc1\n" \
"buffer_load_dwordx4 v[68:71], v144, %[ws], s39 offen offset:1024 sc1\n" \
"buffer_load_dwordx4 v[72:75], v144, %[ws], s39 offen offset:2048 sc1\n" \
"buffer_load_dwordx4 v[76:79], v144, %[ws], s39 offen offset:3072 sc1\n" \
"buffer_load_dwordx4 v[80:83], v145, %[ws], s39 offen offset:0 sc1\n" \
"buffer_load_dwordx4 v[84:87], v145, %[ws], s39 offen offset:1024 sc1\n" \
"buffer_load_dwordx4 v[88:91], v145, %[ws], s39 offen offset:2048 sc1\n" \
"buffer_load_dwordx4 v[92:95], v145, %[ws], s39 offen offset:3072 sc1\n" \
"buffer_load_dwordx4 v[96:99], v146, %[ws], s39 offen offset:0 sc1\n" \
"buffer_load_dwordx4 v[100:103], v146, %[ws], s39 offen offset:1024 sc1\n" \
"buffer_load_dwordx4 v[104:107], v146, %[ws], s39 offen offset:2048 sc1\n" \
"buffer_load_dwordx4 v[108:111], v146, %[ws], s39 offen offset:3072 sc1\n" \
"buffer_load_dwordx4 v[112:115], v147, %[ws], s39 offen offset:0 sc1\n" \
"buffer_load_dwordx4 v[116:119], v147, %[ws], s39 offen offset:1024 sc1\n" \
"buffer_load_dwordx4 v[120:123], v147, %[ws], s39 offen offset:2048 sc1\n" \
"buffer_load_dwordx4 v[124:127], v147, %[ws], s39 offen offset:3072 sc1\n" \
".Lo_s11_%=:\n" \
"s_add_u32 s42, s36, 1\n" \
"v_cmp_ge_i32 vcc, s42, v136\n" \
"s_andn2_b64 vcc, vcc, s[44:45]\n" \
"s_cbranch_scc1 .Lo_n11_%=\n" \
"s_add_u32 s39, s36, 1\n" \
"s_lshl_b32 s39, s39, 18\n" \
"s_add_u32 s39, s39, 41943040\n" \
"s_add_u32 s42, s36, 2\n" \
"v_cmp_ge_i32 vcc, s42, v136\n" \
"s_and_b64 vcc, vcc, s[44:45]\n" \
"s_cbranch_scc1 .Lo_c11_%=\n" \
"s_add_u32 s40, s36, 2\n" \
"s_lshl_b32 s40, s40, 18\n" \
"s_add_u32 s40, s40, 41943040\n" \
"buffer_load_dwordx4 v[136:139], v152, %[ws], %[fo] offen sc1\n" \
"v_mov_b64 v[128:129], 0\n" \
"v_mov_b64 v[130:131], 0\n" \
"v_mov_b64 v[132:133], 0\n" \
"v_mov_b64 v[134:135], 0\n" \
"s_nop 0\n" \
"s_waitcnt vmcnt(15)\n" \
"v_smfmac_f32_16x16x64_bf16 v[128:131], v[64:67], a[0:7], v150\n" \
"v_smfmac_f32_16x16x64_bf16 v[132:135], v[64:67], a[8:15], v150\n" \
"buffer_load_dwordx4 v[0:3], v144, %[ws], s39 offen offset:0 sc1\n" \
"buffer_load_dwordx4 v[4:7], v144, %[ws], s39 offen offset:1024 sc1\n" \
"s_waitcnt vmcnt(16)\n" \
"v_smfmac_f32_16x16x64_bf16 v[128:131], v[68:71], a[16:23], v150\n" \
"v_smfmac_f32_16x16x64_bf16 v[132:135], v[68:71], a[24:31], v150\n" \
"buffer_load_dwordx4 v[8:11], v144, %[ws], s39 offen offset:2048 sc1\n" \
"buffer_load_dwordx4 v[12:15], v144, %[ws], s39 offen offset:3072 sc1\n" \
"s_waitcnt vmcnt(17)\n" \
"v_smfmac_f32_16x16x64_bf16 v[128:131], v[72:75], a[32:39], v150\n" \
"v_smfmac_f32_16x16x64_bf16 v[132:135], v[72:75], a[40:47], v150\n" \
"buffer_load_dwordx4 v[16:19], v145, %[ws], s39 offen offset:0 sc1\n" \
"buffer_load_dwordx4 v[20:23], v145, %[ws], s39 offen offset:1024 sc1\n" \
"s_cmp_eq_u32 s37, 0\n" \
"s_cbranch_scc1 .Lo_d11f_%=\n" \
"s_waitcnt lgkmcnt(0)\n" \
"s_barrier\n" \
"ds_read_b32 v140, v154 offset:0\n" \
"ds_read_b32 v141, v154 offset:1024\n" \
"ds_read_b32 v142, v154 offset:2048\n" \
"ds_read_b32 v143, v154 offset:3072\n" \
"v_xor_b32 v154, 4096, v154\n" \
"s_waitcnt lgkmcnt(0)\n" \
"v_add_f32 v140, v140, v141\n" \
"v_add_f32 v142, v142, v143\n" \
"v_add_f32 v140, v140, v142\n" \
"v_cvt_pk_bf16_f32 v140, 0, v140\n" \
"buffer_store_dword v140, v153, %[out], s38 offen sc1\n" \
"s_add_u32 s38, s38, 131072\n" \
".Lo_d11f_%=:\n" \
"s_waitcnt vmcnt(18)\n" \
"v_smfmac_f32_16x16x64_bf16 v[128:131], v[76:79], a[48:55], v150\n" \
"v_smfmac_f32_16x16x64_bf16 v[132:135], v[76:79], a[56:63], v150\n" \
"buffer_load_dwordx4 v[24:27], v145, %[ws], s39 offen offset:2048 sc1\n" \
"buffer_load_dwordx4 v[28:31], v145, %[ws], s39 offen offset:3072 sc1\n" \
"s_waitcnt vmcnt(19)\n" \
"v_smfmac_f32_16x16x64_bf16 v[128:131], v[80:83], a[64:71], v150\n" \
"v_smfmac_f32_16x16x64_bf16 v[132:135], v[80:83], a[72:79], v150\n" \
"buffer_load_dwordx4 v[32:35], v146, %[ws], s39 offen offset:0 sc1\n" \
"buffer_load_dwordx4 v[36:39], v146, %[ws], s39 offen offset:1024 sc1\n" \
"s_waitcnt vmcnt(20)\n" \
"v_smfmac_f32_16x16x64_bf16 v[128:131], v[84:87], a[80:87], v150\n" \
"v_smfmac_f32_16x16x64_bf16 v[132:135], v[84:87], a[88:95], v150\n" \
"buffer_load_dwordx4 v[40:43], v146, %[ws], s39 offen offset:2048 sc1\n" \
"buffer_load_dwordx4 v[44:47], v146, %[ws], s39 offen offset:3072 sc1\n" \
"s_waitcnt vmcnt(21)\n" \
"v_smfmac_f32_16x16x64_bf16 v[128:131], v[88:91], a[96:103], v150\n" \
"v_smfmac_f32_16x16x64_bf16 v[132:135], v[88:91], a[104:111], v150\n" \
"buffer_load_dwordx4 v[48:51], v147, %[ws], s39 offen offset:0 sc1\n" \
"buffer_load_dwordx4 v[52:55], v147, %[ws], s39 offen offset:1024 sc1\n" \
"s_waitcnt vmcnt(22)\n" \
"v_smfmac_f32_16x16x64_bf16 v[128:131], v[92:95], a[112:119], v150\n" \
"v_smfmac_f32_16x16x64_bf16 v[132:135], v[92:95], a[120:127], v150\n" \
"buffer_load_dwordx4 v[56:59], v147, %[ws], s39 offen offset:2048 sc1\n" \
"buffer_load_dwordx4 v[60:63], v147, %[ws], s39 offen offset:3072 sc1\n" \
"s_waitcnt vmcnt(23)\n" \
"v_smfmac_f32_16x16x64_bf16 v[128:131], v[96:99], a[128:135], v150\n" \
"v_smfmac_f32_16x16x64_bf16 v[132:135], v[96:99], a[136:143], v150\n" \
"buffer_load_dwordx4 v[64:67], v144, %[ws], s40 offen offset:0 sc1\n" \
"buffer_load_dwordx4 v[68:71], v144, %[ws], s40 offen offset:1024 sc1\n" \
"s_waitcnt vmcnt(24)\n" \
"v_smfmac_f32_16x16x64_bf16 v[128:131], v[100:103], a[144:151], v150\n" \
"v_smfmac_f32_16x16x64_bf16 v[132:135], v[100:103], a[152:159], v150\n" \
"buffer_load_dwordx4 v[72:75], v144, %[ws], s40 offen offset:2048 sc1\n" \
"buffer_load_dwordx4 v[76:79], v144, %[ws], s40 offen offset:3072 sc1\n" \
"s_waitcnt vmcnt(25)\n" \
"v_smfmac_f32_16x16x64_bf16 v[128:131], v[104:107], a[160:167], v150\n" \
"v_smfmac_f32_16x16x64_bf16 v[132:135], v[104:107], a[168:175], v150\n" \
"buffer_load_dwordx4 v[80:83], v145, %[ws], s40 offen offset:0 sc1\n" \
"buffer_load_dwordx4 v[84:87], v145, %[ws], s40 offen offset:1024 sc1\n" \
"s_waitcnt vmcnt(26)\n" \
"v_smfmac_f32_16x16x64_bf16 v[128:131], v[108:111], a[176:183], v150\n" \
"v_smfmac_f32_16x16x64_bf16 v[132:135], v[108:111], a[184:191], v150\n" \
"buffer_load_dwordx4 v[88:91], v145, %[ws], s40 offen offset:2048 sc1\n" \
"buffer_load_dwordx4 v[92:95], v145, %[ws], s40 offen offset:3072 sc1\n" \
"s_waitcnt vmcnt(27)\n" \
"v_smfmac_f32_16x16x64_bf16 v[128:131], v[112:115], a[192:199], v150\n" \
"v_smfmac_f32_16x16x64_bf16 v[132:135], v[112:115], a[200:207], v150\n" \
"buffer_load_dwordx4 v[96:99], v146, %[ws], s40 offen offset:0 sc1\n" \
"buffer_load_dwordx4 v[100:103], v146, %[ws], s40 offen offset:1024 sc1\n" \
"s_waitcnt vmcnt(28)\n" \
"v_smfmac_f32_16x16x64_bf16 v[128:131], v[116:119], a[208:215], v150\n" \
"v_smfmac_f32_16x16x64_bf16 v[132:135], v[116:119], a[216:223], v150\n" \
"buffer_load_dwordx4 v[104:107], v146, %[ws], s40 offen offset:2048 sc1\n" \
"buffer_load_dwordx4 v[108:111], v146, %[ws], s40 offen offset:3072 sc1\n" \
"s_waitcnt vmcnt(29)\n" \
"v_smfmac_f32_16x16x64_bf16 v[128:131], v[120:123], a[224:231], v150\n" \
"v_smfmac_f32_16x16x64_bf16 v[132:135], v[120:123], a[232:239], v150\n" \
"buffer_load_dwordx4 v[112:115], v147, %[ws], s40 offen offset:0 sc1\n" \
"buffer_load_dwordx4 v[116:119], v147, %[ws], s40 offen offset:1024 sc1\n" \
"s_waitcnt vmcnt(30)\n" \
"v_smfmac_f32_16x16x64_bf16 v[128:131], v[124:127], a[240:247], v150\n" \
"v_smfmac_f32_16x16x64_bf16 v[132:135], v[124:127], a[248:255], v150\n" \
"buffer_load_dwordx4 v[120:123], v147, %[ws], s40 offen offset:2048 sc1\n" \
"buffer_load_dwordx4 v[124:127], v147, %[ws], s40 offen offset:3072 sc1\n" \
"s_nop 7\n" \
"s_nop 1\n" \
"v_permlane32_swap_b32 v128, v132\n" \
"v_permlane32_swap_b32 v129, v133\n" \
"v_permlane32_swap_b32 v130, v134\n" \
"v_permlane32_swap_b32 v131, v135\n" \
"s_nop 0\n" \
"v_pk_add_f32 v[128:129], v[128:129], v[132:133]\n" \
"v_pk_add_f32 v[130:131], v[130:131], v[134:135]\n" \
"s_nop 0\n" \
"ds_write_b128 v155, v[128:131]\n" \
"v_xor_b32 v155, 4096, v155\n" \
"s_add_u32 s36, s36, 1\n" \
"s_not_b64 s[44:45], s[44:45]\n" \
"s_mov_b32 s37, 1\n" \
"s_waitcnt vmcnt(32)\n" \
"v_min3_i32 v136, v136, v137, v138\n" \
"v_min_i32 v136, v136, v139\n" \
"s_branch .Lo_s20_%=\n" \
".Lo_c11_%=:\n" \
"buffer_load_dwordx4 v[136:139], v152, %[ws], %[fo] offen sc1\n" \
"v_mov_b64 v[128:129], 0\n" \
"v_mov_b64 v[130:131], 0\n" \
"v_mov_b64 v[132:133], 0\n" \
"v_mov_b64 v[134:135], 0\n" \
"s_nop 0\n" \
"s_waitcnt vmcnt(15)\n" \
"v_smfmac_f32_16x16x64_bf16 v[128:131], v[64:67], a[0:7], v150\n" \
"v_smfmac_f32_16x16x64_bf16 v[132:135], v[64:67], a[8:15], v150\n" \
"buffer_load_dwordx4 v[0:3], v144, %[ws], s39 offen offset:0 sc1\n" \
"s_waitcnt vmcnt(15)\n" \
"v_smfmac_f32_16x16x64_bf16 v[128:131], v[68:71], a[16:23], v150\n" \
"v_smfmac_f32_16x16x64_bf16 v[132:135], v[68:71], a[24:31], v150\n" \
"buffer_load_dwordx4 v[4:7], v144, %[ws], s39 offen offset:1024 sc1\n" \
"s_waitcnt vmcnt(15)\n" \
"v_smfmac_f32_16x16x64_bf16 v[128:131], v[72:75], a[32:39], v150\n" \
"v_smfmac_f32_16x16x64_bf16 v[132:135], v[72:75], a[40:47], v150\n" \
"buffer_load_dwordx4 v[8:11], v144, %[ws], s39 offen offset:2048 sc1\n" \
"s_cmp_eq_u32 s37, 0\n" \
"s_cbranch_scc1 .Lo_c11f_%=\n" \
"s_waitcnt lgkmcnt(0)\n" \
"s_barrier\n" \
"ds_read_b32 v140, v154 offset:0\n" \
"ds_read_b32 v141, v154 offset:1024\n" \
"ds_read_b32 v142, v154 offset:2048\n" \
"ds_read_b32 v143, v154 offset:3072\n" \
"v_xor_b32 v154, 4096, v154\n" \
"s_waitcnt lgkmcnt(0)\n" \
"v_add_f32 v140, v140, v141\n" \
"v_add_f32 v142, v142, v143\n" \
"v_add_f32 v140, v140, v142\n" \
"v_cvt_pk_bf16_f32 v140, 0, v140\n" \
"buffer_store_dword v140, v153, %[out], s38 offen sc1\n" \
"s_add_u32 s38, s38, 131072\n" \
".Lo_c11f_%=:\n" \
"s_waitcnt vmcnt(15)\n" \
"v_smfmac_f32_16x16x64_bf16 v[128:131], v[76:79], a[48:55], v150\n" \
"v_smfmac_f32_16x16x64_bf16 v[132:135], v[76:79], a[56:63], v150\n" \
"buffer_load_dwordx4 v[12:15], v144, %[ws], s39 offen offset:3072 sc1\n" \
"s_waitcnt vmcnt(15)\n" \
"v_smfmac_f32_16x16x64_bf16 v[128:131], v[80:83], a[64:71], v150\n" \
"v_smfmac_f32_16x16x64_bf16 v[132:135], v[80:83], a[72:79], v150\n" \
"buffer_load_dwordx4 v[16:19], v145, %[ws], s39 offen offset:0 sc1\n" \
"s_waitcnt vmcnt(15)\n" \
"v_smfmac_f32_16x16x64_bf16 v[128:131], v[84:87], a[80:87], v150\n" \
"v_smfmac_f32_16x16x64_bf16 v[132:135], v[84:87], a[88:95], v150\n" \
"buffer_load_dwordx4 v[20:23], v145, %[ws], s39 offen offset:1024 sc1\n" \
"s_waitcnt vmcnt(15)\n" \
"v_smfmac_f32_16x16x64_bf16 v[128:131], v[88:91], a[96:103], v150\n" \
"v_smfmac_f32_16x16x64_bf16 v[132:135], v[88:91], a[104:111], v150\n" \
"buffer_load_dwordx4 v[24:27], v145, %[ws], s39 offen offset:2048 sc1\n" \
"s_waitcnt vmcnt(15)\n" \
"v_smfmac_f32_16x16x64_bf16 v[128:131], v[92:95], a[112:119], v150\n" \
"v_smfmac_f32_16x16x64_bf16 v[132:135], v[92:95], a[120:127], v150\n" \
"buffer_load_dwordx4 v[28:31], v145, %[ws], s39 offen offset:3072 sc1\n" \
"s_waitcnt vmcnt(15)\n" \
"v_smfmac_f32_16x16x64_bf16 v[128:131], v[96:99], a[128:135], v150\n" \
"v_smfmac_f32_16x16x64_bf16 v[132:135], v[96:99], a[136:143], v150\n" \
"buffer_load_dwordx4 v[32:35], v146, %[ws], s39 offen offset:0 sc1\n" \
"s_waitcnt vmcnt(15)\n" \
"v_smfmac_f32_16x16x64_bf16 v[128:131], v[100:103], a[144:151], v150\n" \
"v_smfmac_f32_16x16x64_bf16 v[132:135], v[100:103], a[152:159], v150\n" \
"buffer_load_dwordx4 v[36:39], v146, %[ws], s39 offen offset:1024 sc1\n" \
"s_waitcnt vmcnt(15)\n" \
"v_smfmac_f32_16x16x64_bf16 v[128:131], v[104:107], a[160:167], v150\n" \
"v_smfmac_f32_16x16x64_bf16 v[132:135], v[104:107], a[168:175], v150\n" \
"buffer_load_dwordx4 v[40:43], v146, %[ws], s39 offen offset:2048 sc1\n" \
"s_waitcnt vmcnt(15)\n" \
"v_smfmac_f32_16x16x64_bf16 v[128:131], v[108:111], a[176:183], v150\n" \
"v_smfmac_f32_16x16x64_bf16 v[132:135], v[108:111], a[184:191], v150\n" \
"buffer_load_dwordx4 v[44:47], v146, %[ws], s39 offen offset:3072 sc1\n" \
"s_waitcnt vmcnt(15)\n" \
"v_smfmac_f32_16x16x64_bf16 v[128:131], v[112:115], a[192:199], v150\n" \
"v_smfmac_f32_16x16x64_bf16 v[132:135], v[112:115], a[200:207], v150\n" \
"buffer_load_dwordx4 v[48:51], v147, %[ws], s39 offen offset:0 sc1\n" \
"s_waitcnt vmcnt(15)\n" \
"v_smfmac_f32_16x16x64_bf16 v[128:131], v[116:119], a[208:215], v150\n" \
"v_smfmac_f32_16x16x64_bf16 v[132:135], v[116:119], a[216:223], v150\n" \
"buffer_load_dwordx4 v[52:55], v147, %[ws], s39 offen offset:1024 sc1\n" \
"s_waitcnt vmcnt(15)\n" \
"v_smfmac_f32_16x16x64_bf16 v[128:131], v[120:123], a[224:231], v150\n" \
"v_smfmac_f32_16x16x64_bf16 v[132:135], v[120:123], a[232:239], v150\n" \
"buffer_load_dwordx4 v[56:59], v147, %[ws], s39 offen offset:2048 sc1\n" \
"s_waitcnt vmcnt(15)\n" \
"v_smfmac_f32_16x16x64_bf16 v[128:131], v[124:127], a[240:247], v150\n" \
"v_smfmac_f32_16x16x64_bf16 v[132:135], v[124:127], a[248:255], v150\n" \
"buffer_load_dwordx4 v[60:63], v147, %[ws], s39 offen offset:3072 sc1\n" \
"s_nop 7\n" \
"s_nop 1\n" \
"v_permlane32_swap_b32 v128, v132\n" \
"v_permlane32_swap_b32 v129, v133\n" \
"v_permlane32_swap_b32 v130, v134\n" \
"v_permlane32_swap_b32 v131, v135\n" \
"s_nop 0\n" \
"v_pk_add_f32 v[128:129], v[128:129], v[132:133]\n" \
"v_pk_add_f32 v[130:131], v[130:131], v[134:135]\n" \
"s_nop 0\n" \
"ds_write_b128 v155, v[128:131]\n" \
"v_xor_b32 v155, 4096, v155\n" \
"s_add_u32 s36, s36, 1\n" \
"s_not_b64 s[44:45], s[44:45]\n" \
"s_mov_b32 s37, 1\n" \
"s_waitcnt vmcnt(16)\n" \
"v_min3_i32 v136, v136, v137, v138\n" \
"v_min_i32 v136, v136, v139\n" \
"s_branch .Lo_s10_%=\n" \
".Lo_n11_%=:\n" \
"buffer_load_dwordx4 v[136:139], v152, %[ws], %[fo] offen sc1\n" \
"v_mov_b64 v[128:129], 0\n" \
"v_mov_b64 v[130:131], 0\n" \
"v_mov_b64 v[132:133], 0\n" \
"v_mov_b64 v[134:135], 0\n" \
"s_nop 0\n" \
"s_waitcnt vmcnt(15)\n" \
"v_smfmac_f32_16x16x64_bf16 v[128:131], v[64:67], a[0:7], v150\n" \
"v_smfmac_f32_16x16x64_bf16 v[132:135], v[64:67], a[8:15], v150\n" \
"s_waitcnt vmcnt(14)\n" \
"v_smfmac_f32_16x16x64_bf16 v[128:131], v[68:71], a[16:23], v150\n" \
"v_smfmac_f32_16x16x64_bf16 v[132:135], v[68:71], a[24:31], v150\n" \
"s_waitcnt vmcnt(13)\n" \
"v_smfmac_f32_16x16x64_bf16 v[128:131], v[72:75], a[32:39], v150\n" \
"v_smfmac_f32_16x16x64_bf16 v[132:135], v[72:75], a[40:47], v150\n" \
"s_cmp_eq_u32 s37, 0\n" \
"s_cbranch_scc1 .Lo_n11f_%=\n" \
"s_waitcnt lgkmcnt(0)\n" \
"s_barrier\n" \
"ds_read_b32 v140, v154 offset:0\n" \
"ds_read_b32 v141, v154 offset:1024\n" \
"ds_read_b32 v142, v154 offset:2048\n" \
"ds_read_b32 v143, v154 offset:3072\n" \
"v_xor_b32 v154, 4096, v154\n" \
"s_waitcnt lgkmcnt(0)\n" \
"v_add_f32 v140, v140, v141\n" \
"v_add_f32 v142, v142, v143\n" \
"v_add_f32 v140, v140, v142\n" \
"v_cvt_pk_bf16_f32 v140, 0, v140\n" \
"buffer_store_dword v140, v153, %[out], s38 offen sc1\n" \
"s_add_u32 s38, s38, 131072\n" \
".Lo_n11f_%=:\n" \
"s_waitcnt vmcnt(12)\n" \
"v_smfmac_f32_16x16x64_bf16 v[128:131], v[76:79], a[48:55], v150\n" \
"v_smfmac_f32_16x16x64_bf16 v[132:135], v[76:79], a[56:63], v150\n" \
"s_waitcnt vmcnt(11)\n" \
"v_smfmac_f32_16x16x64_bf16 v[128:131], v[80:83], a[64:71], v150\n" \
"v_smfmac_f32_16x16x64_bf16 v[132:135], v[80:83], a[72:79], v150\n" \
"s_waitcnt vmcnt(10)\n" \
"v_smfmac_f32_16x16x64_bf16 v[128:131], v[84:87], a[80:87], v150\n" \
"v_smfmac_f32_16x16x64_bf16 v[132:135], v[84:87], a[88:95], v150\n" \
"s_waitcnt vmcnt(9)\n" \
"v_smfmac_f32_16x16x64_bf16 v[128:131], v[88:91], a[96:103], v150\n" \
"v_smfmac_f32_16x16x64_bf16 v[132:135], v[88:91], a[104:111], v150\n" \
"s_waitcnt vmcnt(8)\n" \
"v_smfmac_f32_16x16x64_bf16 v[128:131], v[92:95], a[112:119], v150\n" \
"v_smfmac_f32_16x16x64_bf16 v[132:135], v[92:95], a[120:127], v150\n" \
"s_waitcnt vmcnt(7)\n" \
"v_smfmac_f32_16x16x64_bf16 v[128:131], v[96:99], a[128:135], v150\n" \
"v_smfmac_f32_16x16x64_bf16 v[132:135], v[96:99], a[136:143], v150\n" \
"s_waitcnt vmcnt(6)\n" \
"v_smfmac_f32_16x16x64_bf16 v[128:131], v[100:103], a[144:151], v150\n" \
"v_smfmac_f32_16x16x64_bf16 v[132:135], v[100:103], a[152:159], v150\n" \
"s_waitcnt vmcnt(5)\n" \
"v_smfmac_f32_16x16x64_bf16 v[128:131], v[104:107], a[160:167], v150\n" \
"v_smfmac_f32_16x16x64_bf16 v[132:135], v[104:107], a[168:175], v150\n" \
"s_waitcnt vmcnt(4)\n" \
"v_smfmac_f32_16x16x64_bf16 v[128:131], v[108:111], a[176:183], v150\n" \
"v_smfmac_f32_16x16x64_bf16 v[132:135], v[108:111], a[184:191], v150\n" \
"s_waitcnt vmcnt(3)\n" \
"v_smfmac_f32_16x16x64_bf16 v[128:131], v[112:115], a[192:199], v150\n" \
"v_smfmac_f32_16x16x64_bf16 v[132:135], v[112:115], a[200:207], v150\n" \
"s_waitcnt vmcnt(2)\n" \
"v_smfmac_f32_16x16x64_bf16 v[128:131], v[116:119], a[208:215], v150\n" \
"v_smfmac_f32_16x16x64_bf16 v[132:135], v[116:119], a[216:223], v150\n" \
"s_waitcnt vmcnt(1)\n" \
"v_smfmac_f32_16x16x64_bf16 v[128:131], v[120:123], a[224:231], v150\n" \
"v_smfmac_f32_16x16x64_bf16 v[132:135], v[120:123], a[232:239], v150\n" \
"s_waitcnt vmcnt(0)\n" \
"v_smfmac_f32_16x16x64_bf16 v[128:131], v[124:127], a[240:247], v150\n" \
"v_smfmac_f32_16x16x64_bf16 v[132:135], v[124:127], a[248:255], v150\n" \
"s_nop 7\n" \
"s_nop 1\n" \
"v_permlane32_swap_b32 v128, v132\n" \
"v_permlane32_swap_b32 v129, v133\n" \
"v_permlane32_swap_b32 v130, v134\n" \
"v_permlane32_swap_b32 v131, v135\n" \
"s_nop 0\n" \
"v_pk_add_f32 v[128:129], v[128:129], v[132:133]\n" \
"v_pk_add_f32 v[130:131], v[130:131], v[134:135]\n" \
"s_nop 0\n" \
"ds_write_b128 v155, v[128:131]\n" \
"v_xor_b32 v155, 4096, v155\n" \
"s_add_u32 s36, s36, 1\n" \
"s_not_b64 s[44:45], s[44:45]\n" \
"s_cmp_eq_u32 s37, 0\n" \
"s_cbranch_scc1 .Lo_w01_%=\n" \
"s_waitcnt vmcnt(1)\n" \
"s_branch .Lo_w11_%=\n" \
".Lo_w01_%=:\n" \
"s_waitcnt vmcnt(0)\n" \
".Lo_w11_%=:\n" \
"v_min3_i32 v136, v136, v137, v138\n" \
"v_min_i32 v136, v136, v139\n" \
"s_mov_b32 s37, 1\n" \
"s_add_u32 s42, s36, 0\n" \
"v_cmp_ge_i32 vcc, s42, v136\n" \
"s_and_b64 vcc, vcc, s[44:45]\n" \
"s_cbranch_scc1 .Lo_dry1_%=\n" \
"s_branch .Lo_get1_%=\n" \
".Lo_dry1_%=:\n" \
"s_waitcnt lgkmcnt(0)\n" \
"s_barrier\n" \
"ds_read_b32 v140, v154 offset:0\n" \
"ds_read_b32 v141, v154 offset:1024\n" \
"ds_read_b32 v142, v154 offset:2048\n" \
"ds_read_b32 v143, v154 offset:3072\n" \
"v_xor_b32 v154, 4096, v154\n" \
"s_waitcnt lgkmcnt(0)\n" \
"v_add_f32 v140, v140, v141\n" \
"v_add_f32 v142, v142, v143\n" \
"v_add_f32 v140, v140, v142\n" \
"v_cvt_pk_bf16_f32 v140, 0, v140\n" \
"buffer_store_dword v140, v153, %[out], s38 offen sc1\n" \
"s_add_u32 s38, s38, 131072\n" \
"s_cmp_eq_u32 s36, 64\n" \
"s_cbranch_scc1 .Lo_end_%=\n" \
"s_branch .Lo_poll1_%=\n" \
".Lo_idle1_%=:\n" \
"s_sub_u32 s41, s41, 1\n" \
"s_cbranch_scc1 .Lo_end_%=\n" \
"s_cmp_ge_u32 s36, 62\n" \
"s_cbranch_scc1 .Lo_poll1_%=\n" \
"s_sleep 8\n" \
"s_branch .Lo_poll1_%=\n" \
".Lo_s21_%=:\n" \
"s_add_u32 s42, s36, 2\n" \
"v_cmp_ge_i32 vcc, s42, v136\n" \
"s_and_b64 vcc, vcc, s[44:45]\n" \
"s_cbranch_scc1 .Lo_n21_%=\n" \
"s_add_u32 s40, s36, 2\n" \
"s_lshl_b32 s40, s40, 18\n" \
"s_add_u32 s40, s40, 41943040\n" \
"buffer_load_dwordx4 v[136:139], v152, %[ws], %[fo] offen sc1\n" \
"v_mov_b64 v[128:129], 0\n" \
"v_mov_b64 v[130:131], 0\n" \
"v_mov_b64 v[132:133], 0\n" \
"v_mov_b64 v[134:135], 0\n" \
"s_nop 0\n" \
"s_waitcnt vmcnt(31)\n" \
"v_smfmac_f32_16x16x64_bf16 v[128:131], v[64:67], a[0:7], v150\n" \
"v_smfmac_f32_16x16x64_bf16 v[132:135], v[64:67], a[8:15], v150\n" \
"buffer_load_dwordx4 v[64:67], v144, %[ws], s40 offen offset:0 sc1\n" \
"s_waitcnt vmcnt(31)\n" \
"v_smfmac_f32_16x16x64_bf16 v[128:131], v[68:71], a[16:23], v150\n" \
"v_smfmac_f32_16x16x64_bf16 v[132:135], v[68:71], a[24:31], v150\n" \
"buffer_load_dwordx4 v[68:71], v144, %[ws], s40 offen offset:1024 sc1\n" \
"s_waitcnt vmcnt(31)\n" \
"v_smfmac_f32_16x16x64_bf16 v[128:131], v[72:75], a[32:39], v150\n" \
"v_smfmac_f32_16x16x64_bf16 v[132:135], v[72:75], a[40:47], v150\n" \
"buffer_load_dwordx4 v[72:75], v144, %[ws], s40 offen offset:2048 sc1\n" \
"s_waitcnt lgkmcnt(0)\n" \
"s_barrier\n" \
"ds_read_b32 v140, v154 offset:0\n" \
"ds_read_b32 v141, v154 offset:1024\n" \
"ds_read_b32 v142, v154 offset:2048\n" \
"ds_read_b32 v143, v154 offset:3072\n" \
"v_xor_b32 v154, 4096, v154\n" \
"s_waitcnt lgkmcnt(0)\n" \
"v_add_f32 v140, v140, v141\n" \
"v_add_f32 v142, v142, v143\n" \
"v_add_f32 v140, v140, v142\n" \
"v_cvt_pk_bf16_f32 v140, 0, v140\n" \
"buffer_store_dword v140, v153, %[out], s38 offen sc1\n" \
"s_add_u32 s38, s38, 131072\n" \
"s_waitcnt vmcnt(31)\n" \
"v_smfmac_f32_16x16x64_bf16 v[128:131], v[76:79], a[48:55], v150\n" \
"v_smfmac_f32_16x16x64_bf16 v[132:135], v[76:79], a[56:63], v150\n" \
"buffer_load_dwordx4 v[76:79], v144, %[ws], s40 offen offset:3072 sc1\n" \
"s_waitcnt vmcnt(31)\n" \
"v_smfmac_f32_16x16x64_bf16 v[128:131], v[80:83], a[64:71], v150\n" \
"v_smfmac_f32_16x16x64_bf16 v[132:135], v[80:83], a[72:79], v150\n" \
"buffer_load_dwordx4 v[80:83], v145, %[ws], s40 offen offset:0 sc1\n" \
"s_waitcnt vmcnt(31)\n" \
"v_smfmac_f32_16x16x64_bf16 v[128:131], v[84:87], a[80:87], v150\n" \
"v_smfmac_f32_16x16x64_bf16 v[132:135], v[84:87], a[88:95], v150\n" \
"buffer_load_dwordx4 v[84:87], v145, %[ws], s40 offen offset:1024 sc1\n" \
"s_waitcnt vmcnt(31)\n" \
"v_smfmac_f32_16x16x64_bf16 v[128:131], v[88:91], a[96:103], v150\n" \
"v_smfmac_f32_16x16x64_bf16 v[132:135], v[88:91], a[104:111], v150\n" \
"buffer_load_dwordx4 v[88:91], v145, %[ws], s40 offen offset:2048 sc1\n" \
"s_waitcnt vmcnt(31)\n" \
"v_smfmac_f32_16x16x64_bf16 v[128:131], v[92:95], a[112:119], v150\n" \
"v_smfmac_f32_16x16x64_bf16 v[132:135], v[92:95], a[120:127], v150\n" \
"buffer_load_dwordx4 v[92:95], v145, %[ws], s40 offen offset:3072 sc1\n" \
"s_waitcnt vmcnt(31)\n" \
"v_smfmac_f32_16x16x64_bf16 v[128:131], v[96:99], a[128:135], v150\n" \
"v_smfmac_f32_16x16x64_bf16 v[132:135], v[96:99], a[136:143], v150\n" \
"buffer_load_dwordx4 v[96:99], v146, %[ws], s40 offen offset:0 sc1\n" \
"s_waitcnt vmcnt(31)\n" \
"v_smfmac_f32_16x16x64_bf16 v[128:131], v[100:103], a[144:151], v150\n" \
"v_smfmac_f32_16x16x64_bf16 v[132:135], v[100:103], a[152:159], v150\n" \
"buffer_load_dwordx4 v[100:103], v146, %[ws], s40 offen offset:1024 sc1\n" \
"s_waitcnt vmcnt(31)\n" \
"v_smfmac_f32_16x16x64_bf16 v[128:131], v[104:107], a[160:167], v150\n" \
"v_smfmac_f32_16x16x64_bf16 v[132:135], v[104:107], a[168:175], v150\n" \
"buffer_load_dwordx4 v[104:107], v146, %[ws], s40 offen offset:2048 sc1\n" \
"s_waitcnt vmcnt(31)\n" \
"v_smfmac_f32_16x16x64_bf16 v[128:131], v[108:111], a[176:183], v150\n" \
"v_smfmac_f32_16x16x64_bf16 v[132:135], v[108:111], a[184:191], v150\n" \
"buffer_load_dwordx4 v[108:111], v146, %[ws], s40 offen offset:3072 sc1\n" \
"s_waitcnt vmcnt(31)\n" \
"v_smfmac_f32_16x16x64_bf16 v[128:131], v[112:115], a[192:199], v150\n" \
"v_smfmac_f32_16x16x64_bf16 v[132:135], v[112:115], a[200:207], v150\n" \
"buffer_load_dwordx4 v[112:115], v147, %[ws], s40 offen offset:0 sc1\n" \
"s_waitcnt vmcnt(31)\n" \
"v_smfmac_f32_16x16x64_bf16 v[128:131], v[116:119], a[208:215], v150\n" \
"v_smfmac_f32_16x16x64_bf16 v[132:135], v[116:119], a[216:223], v150\n" \
"buffer_load_dwordx4 v[116:119], v147, %[ws], s40 offen offset:1024 sc1\n" \
"s_waitcnt vmcnt(31)\n" \
"v_smfmac_f32_16x16x64_bf16 v[128:131], v[120:123], a[224:231], v150\n" \
"v_smfmac_f32_16x16x64_bf16 v[132:135], v[120:123], a[232:239], v150\n" \
"buffer_load_dwordx4 v[120:123], v147, %[ws], s40 offen offset:2048 sc1\n" \
"s_waitcnt vmcnt(31)\n" \
"v_smfmac_f32_16x16x64_bf16 v[128:131], v[124:127], a[240:247], v150\n" \
"v_smfmac_f32_16x16x64_bf16 v[132:135], v[124:127], a[248:255], v150\n" \
"buffer_load_dwordx4 v[124:127], v147, %[ws], s40 offen offset:3072 sc1\n" \
"s_nop 7\n" \
"s_nop 1\n" \
"v_permlane32_swap_b32 v128, v132\n" \
"v_permlane32_swap_b32 v129, v133\n" \
"v_permlane32_swap_b32 v130, v134\n" \
"v_permlane32_swap_b32 v131, v135\n" \
"s_nop 0\n" \
"v_pk_add_f32 v[128:129], v[128:129], v[132:133]\n" \
"v_pk_add_f32 v[130:131], v[130:131], v[134:135]\n" \
"s_nop 0\n" \
"ds_write_b128 v155, v[128:131]\n" \
"v_xor_b32 v155, 4096, v155\n" \
"s_add_u32 s36, s36, 1\n" \
"s_not_b64 s[44:45], s[44:45]\n" \
"s_waitcnt vmcnt(17)\n" \
"v_min3_i32 v136, v136, v137, v138\n" \
"v_min_i32 v136, v136, v139\n" \
"s_branch .Lo_s20_%=\n" \
".Lo_n21_%=:\n" \
"buffer_load_dwordx4 v[136:139], v152, %[ws], %[fo] offen sc1\n" \
"v_mov_b64 v[128:129], 0\n" \
"v_mov_b64 v[130:131], 0\n" \
"v_mov_b64 v[132:133], 0\n" \
"v_mov_b64 v[134:135], 0\n" \
"s_nop 0\n" \
"s_waitcnt vmcnt(31)\n" \
"v_smfmac_f32_16x16x64_bf16 v[128:131], v[64:67], a[0:7], v150\n" \
"v_smfmac_f32_16x16x64_bf16 v[132:135], v[64:67], a[8:15], v150\n" \
"s_waitcnt vmcnt(30)\n" \
"v_smfmac_f32_16x16x64_bf16 v[128:131], v[68:71], a[16:23], v150\n" \
"v_smfmac_f32_16x16x64_bf16 v[132:135], v[68:71], a[24:31], v150\n" \
"s_waitcnt vmcnt(29)\n" \
"v_smfmac_f32_16x16x64_bf16 v[128:131], v[72:75], a[32:39], v150\n" \
"v_smfmac_f32_16x16x64_bf16 v[132:135], v[72:75], a[40:47], v150\n" \
"s_waitcnt lgkmcnt(0)\n" \
"s_barrier\n" \
"ds_read_b32 v140, v154 offset:0\n" \
"ds_read_b32 v141, v154 offset:1024\n" \
"ds_read_b32 v142, v154 offset:2048\n" \
"ds_read_b32 v143, v154 offset:3072\n" \
"v_xor_b32 v154, 4096, v154\n" \
"s_waitcnt lgkmcnt(0)\n" \
"v_add_f32 v140, v140, v141\n" \
"v_add_f32 v142, v142, v143\n" \
"v_add_f32 v140, v140, v142\n" \
"v_cvt_pk_bf16_f32 v140, 0, v140\n" \
"buffer_store_dword v140, v153, %[out], s38 offen sc1\n" \
"s_add_u32 s38, s38, 131072\n" \
"s_waitcnt vmcnt(28)\n" \
"v_smfmac_f32_16x16x64_bf16 v[128:131], v[76:79], a[48:55], v150\n" \
"v_smfmac_f32_16x16x64_bf16 v[132:135], v[76:79], a[56:63], v150\n" \
"s_waitcnt vmcnt(27)\n" \
"v_smfmac_f32_16x16x64_bf16 v[128:131], v[80:83], a[64:71], v150\n" \
"v_smfmac_f32_16x16x64_bf16 v[132:135], v[80:83], a[72:79], v150\n" \
"s_waitcnt vmcnt(26)\n" \
"v_smfmac_f32_16x16x64_bf16 v[128:131], v[84:87], a[80:87], v150\n" \
"v_smfmac_f32_16x16x64_bf16 v[132:135], v[84:87], a[88:95], v150\n" \
"s_waitcnt vmcnt(25)\n" \
"v_smfmac_f32_16x16x64_bf16 v[128:131], v[88:91], a[96:103], v150\n" \
"v_smfmac_f32_16x16x64_bf16 v[132:135], v[88:91], a[104:111], v150\n" \
"s_waitcnt vmcnt(24)\n" \
"v_smfmac_f32_16x16x64_bf16 v[128:131], v[92:95], a[112:119], v150\n" \
"v_smfmac_f32_16x16x64_bf16 v[132:135], v[92:95], a[120:127], v150\n" \
"s_waitcnt vmcnt(23)\n" \
"v_smfmac_f32_16x16x64_bf16 v[128:131], v[96:99], a[128:135], v150\n" \
"v_smfmac_f32_16x16x64_bf16 v[132:135], v[96:99], a[136:143], v150\n" \
"s_waitcnt vmcnt(22)\n" \
"v_smfmac_f32_16x16x64_bf16 v[128:131], v[100:103], a[144:151], v150\n" \
"v_smfmac_f32_16x16x64_bf16 v[132:135], v[100:103], a[152:159], v150\n" \
"s_waitcnt vmcnt(21)\n" \
"v_smfmac_f32_16x16x64_bf16 v[128:131], v[104:107], a[160:167], v150\n" \
"v_smfmac_f32_16x16x64_bf16 v[132:135], v[104:107], a[168:175], v150\n" \
"s_waitcnt vmcnt(20)\n" \
"v_smfmac_f32_16x16x64_bf16 v[128:131], v[108:111], a[176:183], v150\n" \
"v_smfmac_f32_16x16x64_bf16 v[132:135], v[108:111], a[184:191], v150\n" \
"s_waitcnt vmcnt(19)\n" \
"v_smfmac_f32_16x16x64_bf16 v[128:131], v[112:115], a[192:199], v150\n" \
"v_smfmac_f32_16x16x64_bf16 v[132:135], v[112:115], a[200:207], v150\n" \
"s_waitcnt vmcnt(18)\n" \
"v_smfmac_f32_16x16x64_bf16 v[128:131], v[116:119], a[208:215], v150\n" \
"v_smfmac_f32_16x16x64_bf16 v[132:135], v[116:119], a[216:223], v150\n" \
"s_waitcnt vmcnt(17)\n" \
"v_smfmac_f32_16x16x64_bf16 v[128:131], v[120:123], a[224:231], v150\n" \
"v_smfmac_f32_16x16x64_bf16 v[132:135], v[120:123], a[232:239], v150\n" \
"s_waitcnt vmcnt(16)\n" \
"v_smfmac_f32_16x16x64_bf16 v[128:131], v[124:127], a[240:247], v150\n" \
"v_smfmac_f32_16x16x64_bf16 v[132:135], v[124:127], a[248:255], v150\n" \
"s_nop 7\n" \
"s_nop 1\n" \
"v_permlane32_swap_b32 v128, v132\n" \
"v_permlane32_swap_b32 v129, v133\n" \
"v_permlane32_swap_b32 v130, v134\n" \
"v_permlane32_swap_b32 v131, v135\n" \
"s_nop 0\n" \
"v_pk_add_f32 v[128:129], v[128:129], v[132:133]\n" \
"v_pk_add_f32 v[130:131], v[130:131], v[134:135]\n" \
"s_nop 0\n" \
"ds_write_b128 v155, v[128:131]\n" \
"v_xor_b32 v155, 4096, v155\n" \
"s_add_u32 s36, s36, 1\n" \
"s_not_b64 s[44:45], s[44:45]\n" \
"s_waitcnt vmcnt(1)\n" \
"v_min3_i32 v136, v136, v137, v138\n" \
"v_min_i32 v136, v136, v139\n" \
"s_branch .Lo_s10_%=\n" \
".Lo_end_%=:\n"
#define OLOOP_CLOBBERS "v0", "v1", "v2", "v3", "v4", "v5", "v6", "v7", "v8", "v9", "v10", "v11", "v12", "v13", "v14", "v15", "v16", "v17", "v18", "v19", "v20", "v21", "v22", "v23", "v24", "v25", "v26", "v27", "v28", "v29", "v30", "v31", "v32", "v33", "v34", "v35", "v36", "v37", "v38", "v39", "v40", "v41", "v42", "v43", "v44", "v45", "v46", "v47", "v48", "v49", "v50", "v51", "v52", "v53", "v54", "v55", "v56", "v57", "v58", "v59", "v60", "v61", "v62", "v63", "v64", "v65", "v66", "v67", "v68", "v69", "v70", "v71", "v72", "v73", "v74", "v75", "v76", "v77", "v78", "v79", "v80", "v81", "v82", "v83", "v84", "v85", "v86", "v87", "v88", "v89", "v90", "v91", "v92", "v93", "v94", "v95", "v96", "v97", "v98", "v99", "v100", "v101", "v102", "v103", "v104", "v105", "v106", "v107", "v108", "v109", "v110", "v111", "v112", "v113", "v114", "v115", "v116", "v117", "v118", "v119", "v120", "v121", "v122", "v123", "v124", "v125", "v126", "v127", "v128", "v129", "v130", "v131", "v132", "v133", "v134", "v135", "v136", "v137", "v138", "v139", "v140", "v141", "v142", "v143", "v145", "v146", "v147", "s36", "s37", "s38", "s39", "s40", "s41", "s42", "s43", "s44", "s45", "vcc", "scc", "memory"

constexpr unsigned T_DA = 117, T_P = 158, T_Q = 52;

typedef unsigned short u16;
typedef unsigned u32;
typedef __bf16 v2bf __attribute__((ext_vector_type(2)));
typedef __bf16 v4bf __attribute__((ext_vector_type(4)));
typedef __bf16 v8bf __attribute__((ext_vector_type(8)));
typedef __bf16 v16bf __attribute__((ext_vector_type(16)));
typedef float v2f __attribute__((ext_vector_type(2)));
typedef float v4f __attribute__((ext_vector_type(4)));
typedef u32 v2u __attribute__((ext_vector_type(2)));
typedef u32 v4u __attribute__((ext_vector_type(4)));
typedef __amdgpu_buffer_rsrc_t rsrc_t;
#define DEV __device__ __forceinline__
#define LDS(T, off) ((__attribute__((address_space(3))) T*)(u32)(off))
#define BC(T, x) __builtin_bit_cast(T, x)

constexpr u32 SLOT0 = 0, INB0 = 2u << 20, FLAG0 = INB0 + (512u << 10), PROBE = FLAG0 + (256u << 10), OFLAG = FLAG0 + 8192, SFLAG = FLAG0 + 16384, ACK = FLAG0 + 20480;
constexpr u32 NONCE = FLAG0 + 40960, DONE = FLAG0 + 49152, REQ = FLAG0 + 65536, GO = FLAG0 + 98304;
constexpr u32 EST = 6u << 20, KV0 = 8u << 20, OB0 = 40u << 20, WS_BYTES = 64u << 20;
constexpr int SC1 = 16, SPIN = 1 << 18;
constexpr float KSCALE = 0.08838834764831845f * 1.4426950408889634f;

struct Args { const u16 *ax, *lre, *lim, *bre, *bim, *cre, *cim, *ldt, *dsk, *bx, *m0, *wq, *wk, *wv, *wo; float* out; char* ws; };

DEV float b2f(u16 v) { return __uint_as_float((u32)v << 16); }
DEV u32 pk(float a, float b) { return BC(u32, (v2bf{(__bf16)a, (__bf16)b})); }
DEV float rb(float a) { return (float)(__bf16)a; }
DEV v8bf pk8(v4f a, v4f b) { return BC(v8bf, (v4u{pk(a[0], a[1]), pk(a[2], a[3]), pk(b[0], b[1]), pk(b[2], b[3])})); }
DEV rsrc_t rs(const void* p, u32 n) { return __builtin_amdgcn_make_buffer_rsrc((void*)p, 0, n, 0x20000); }
DEV v4f mm(v8bf a, v8bf b, v4f c) { return __builtin_amdgcn_mfma_f32_16x16x32_bf16(a, b, c, 0, 0, 0); }
typedef u32 v8u __attribute__((ext_vector_type(8)));
typedef u32 v16u __attribute__((ext_vector_type(16)));
typedef u32 v32u __attribute__((ext_vector_type(32)));
#define I8(o) o, o + 1, o + 2, o + 3, o + 4, o + 5, o + 6, o + 7
DEV v32u quad(v16bf a, v16bf b, v16bf c, v16bf d) {
    const v16u p = __builtin_shufflevector(BC(v8u, a), BC(v8u, b), I8(0), I8(8)), q = __builtin_shufflevector(BC(v8u, c), BC(v8u, d), I8(0), I8(8));
    return __builtin_shufflevector(p, q, I8(0), I8(8), I8(16), I8(24));
}
DEV v8bf half(v16bf x, int u) { return u ? __builtin_shufflevector(x, x, 8, 9, 10, 11, 12, 13, 14, 15) : __builtin_shufflevector(x, x, 0, 1, 2, 3, 4, 5, 6, 7); }
DEV v4bf trd(u32 off) { return __builtin_amdgcn_ds_read_tr16_b64_v4bf16(LDS(v4bf, off)); }
DEV v8bf cat(v4bf a, v4bf b) { return __builtin_shufflevector(a, b, 0, 1, 2, 3, 4, 5, 6, 7); }
DEV v16bf cat(v8bf a, v8bf b) { return __builtin_shufflevector(a, b, 0, 1, 2, 3, 4, 5, 6, 7, 8, 9, 10, 11, 12, 13, 14, 15); }
DEV void bar() { asm volatile("s_waitcnt lgkmcnt(0)\n s_barrier" ::: "memory"); }
template <int N> DEV void vwait() { asm volatile("s_waitcnt vmcnt(%0)" ::"n"(N) : "memory"); }
DEV void lwait() { asm volatile("s_waitcnt lgkmcnt(0)" ::: "memory"); }
DEV void dma(rsrc_t r, u32 lds, int lo, u32 voff, u32 soff, int io) {
    asm volatile("s_add_u32 m0, %0, %4\n s_nop 0\n buffer_load_dwordx4 %1, %2, %3 offen offset:%5 lds" ::"s"(lds), "v"(voff), "s"(r), "s"(soff), "n"(lo - io), "n"(io) : "memory", "m0", "scc");
}
DEV v4u ld4(rsrc_t r, u32 v, u32 s) { return __builtin_amdgcn_raw_buffer_load_b128(r, v, s, SC1); }
DEV u32 ld1(rsrc_t r, u32 v, u32 s) { return __builtin_amdgcn_raw_buffer_load_b32(r, v, s, SC1); }
DEV void st4(v4u x, rsrc_t r, u32 v, u32 s) { __builtin_amdgcn_raw_buffer_store_b128(x, r, v, s, SC1); }
DEV void st1(u32 x, rsrc_t r, u32 v, u32 s) { __builtin_amdgcn_raw_buffer_store_b32(x, r, v, s, SC1); }
DEV float xor16(float x) { return __shfl_xor(x, 16); }
DEV float xor32(float x) { return __shfl_xor(x, 32); }

template <int NU, int RING, class Src, class Dst, class Hook> DEV void stage(u32 rd, Src src, Dst dst, Hook hook) {
#pragma unroll
    for (int u = 0; u < RING; ++u) src(u, u * 4096);
#pragma unroll
    for (int u = 0; u < NU; ++u) {
        const int left = NU - 1 - u < RING - 1 ? NU - 1 - u : RING - 1;
        switch (left) {
            case 7: vwait<28>(); break; case 6: vwait<24>(); break; case 5: vwait<20>(); break; case 4: vwait<16>(); break;
            case 3: vwait<12>(); break; case 2: vwait<8>(); break; case 1: vwait<4>(); break; default: vwait<0>();
        }
#pragma unroll
        for (int t = 0; t < 2; ++t) { const u32 o = (rd ^ t * 32) + (u % RING) * 4096; dst(u, t, cat(trd(o), trd(o + 1024)), cat(trd(o + 2048), trd(o + 3072))); }
        if (u + RING < NU) { lwait(); src(u + RING, (u % RING) * 4096); }
        hook(u);
    }
}

DEV void orole(const Args& a, char* ws, int grp, int nb, u32 cfg, int w, int l) {
    const int n = l & 15, g = l >> 4;
    const rsrc_t R = rs(ws, WS_BYTES), Wr = rs(a.wo, 4u << 20);
    v16bf W[2][16];
    {
        const u32 vo = (((l >> 2) & 1) + ((l >> 3) & 1) * 32 + (g & 1) * 16 + (l >> 5) * 4) * 1024 + (((l >> 1) ^ g) & 1) * 32 + (l & 1) * 16;
        const u32 ring = 32768 + w * 32768, rd = ring + g * 256 + ((l >> 2) & 3) * 64 + (g & 1) * 32 + (l & 3) * 8;
        stage<16, 8>(rd,
            [&](int u, int dst) {
#pragma unroll
                for (int m = 0; m < 4; ++m) dma(Wr, ring, dst + m * 1024, vo + ((m & 1) * 2 + (m >> 1) * 8) * 1024, (w * 1024 + u * 64) * 1024 + nb * 64, 0); },
            [&](int u, int t, v8bf x, v8bf y) { W[t][u] = cat(x, y); asm volatile("" : "+a"(W[t][u])); }, [](int) {});
    }
    v32u G[8];
#pragma unroll
    for (int i = 0; i < 8; ++i) G[i] = quad(W[0][2 * i], W[1][2 * i], W[0][2 * i + 1], W[1][2 * i + 1]);
    const int tid = w * 64 + l, col = tid & 31, tok = tid >> 5;
    u32 ro = (col >> 4) * 512 + ((tok >> 2) * 16 + (col & 15)) * 16 + (tok & 3) * 4, rw = w * 1024 + l * 16;
    asm volatile(OLOOP_ASM : "+{v154}"(ro), "+{v155}"(rw)
        : "{a[0:31]}"(G[0]), "{a[32:63]}"(G[1]), "{a[64:95]}"(G[2]), "{a[96:127]}"(G[3]), "{a[128:159]}"(G[4]), "{a[160:191]}"(G[5]), "{a[192:223]}"(G[6]), "{a[224:255]}"(G[7]),
          "{v144}"(l * 16 + grp * 65536 + w * 16384), "{v150}"((l & 8) ? 0xEEEE : 0x4444), "{v152}"((l & 7) * 256 + (l & 8) * 16 + w * 2048),
          "{v153}"(((grp * 8 + tok) * 1024 + 512 + nb * 32 + col) * 4), [ws] "s"(R), [out] "s"(rs(a.out, 8u << 20)), [fo] "s"(OFLAG ^ (cfg & 4) << 11)
        : OLOOP_CLOBBERS);
}

struct cf { float x, y; };
DEV float fa(float a, float b, float c) { float d; asm("v_fma_f32 %0, %1, %2, %3" : "=v"(d) : "v"(a), "v"(b), "v"(c)); return d; }
DEV float fs(float a, float b, float c) { float d; asm("v_fma_f32 %0, -%1, %2, %3" : "=v"(d) : "v"(a), "v"(b), "v"(c)); return d; }
DEV cf cmul(cf a, cf b) { return {fs(a.y, b.y, a.x * b.x), fa(a.y, b.x, a.x * b.y)}; }
DEV cf cfma(cf a, cf b, cf c) { return {fa(a.x, b.x, fs(a.y, b.y, c.x)), fa(a.x, b.y, fa(a.y, b.x, c.y))}; }
DEV cf pick(bool p, cf a, cf b) { return {p ? a.x : b.x, p ? a.y : b.y}; }
DEV v4f lam_coef(const Args& a, int p) {
    const float dt = rb(expf(b2f(a.ldt[p]))), re = b2f(a.lre[p]), im = b2f(a.lim[p]), e = expf(dt * re);
    float sn, cs; sincosf(dt * im, &sn, &cs);
    const cf lb = {e * cs, e * sn};
    const float den = re * re + im * im, nr = lb.x - 1.f;
    return v4f{lb.x, lb.y, (nr * re + lb.y * im) / den, (lb.y * re - nr * im) / den};
}
#define SWAP(op, a, b) asm("s_nop 1\n " op " %0, %2\n " op " %1, %3" : "+v"(a.x), "+v"(a.y), "+v"(b.x), "+v"(b.y))
template <int I> DEV cf rows(cf a) { cf b = a; SWAP("v_permlane16_swap_b32", a, b); return I ? b : a; }
DEV cf half0(cf a) { cf b = a; SWAP("v_permlane32_swap_b32", a, b); return a; }
constexpr u32 HI0 = 0, SRING = 65536, SNR = 5, HIN = SRING + 4 * SNR * 4096, LTAB = HIN + 4096;
struct Stream {
    rsrc_t br, bi, cr, ci; u32 v0, ring, wr, rd, ra; int rot;
    DEV void issue(int g2, int kk, int t) {
        const int k = (kk + rot) & (g2 ? 15 : 7);
        const u32 so = (k & 7) * 128 + (g2 ? 2 * t : t) * 16384, l = ring + wr;
        const rsrc_t p = g2 ? (k < 8 ? cr : ci) : br, q = g2 ? p : bi;
        dma(p, l, 0, v0, so, 0); dma(p, l, 1024, (v0 + 8192) ^ 64, so, 0);
        dma(q, l, 2048, v0, so + (g2 ? 16384 : 0), 0); dma(q, l, 3072, (v0 + 8192) ^ 64, so + (g2 ? 16384 : 0), 0);
        wr = wr == (SNR - 1) * 4096 ? 0 : wr + 4096;
    }
    DEV void read(v8bf (&f)[4]) {
#pragma unroll
        for (int i = 0; i < 4; ++i) f[i] = *LDS(v8bf, (ra ^ (i & 1) * 64) + rd + (i >> 1) * 2048);
        rd = rd == (SNR - 1) * 4096 ? 0 : rd + 4096;
    }
};
template <int N> DEV void lwaitn() { asm volatile("s_waitcnt lgkmcnt(%0)" ::"n"(N) : "memory"); }
DEV void srole(const Args& a, char* ws, int c, int w, int l) {
    const int n = l & 15, g = l >> 4;
    const rsrc_t R = rs(ws, WS_BYTES);
    const u32 fa = (n >> 3) * 1024 + (n & 7) * 128 + (g ^ (n >> 1)) * 16, lv = (l >> 3) * 1024 + ((l & 7) ^ g) * 16;
    Stream S{rs(a.bre, 1u << 19), rs(a.bim, 1u << 19), rs(a.cre, 1u << 19), rs(a.cim, 1u << 19), w * 131072 + lv, SRING + w * SNR * 4096, 0, 0, SRING + w * SNR * 4096 + fa, c + 2 * w};
    {
        const rsrc_t X = rs(a.ax, 2u << 20);
        const u32 xv = (8 * (l >> 5) + ((l >> 3) & 3)) * 1024 + ((l & 7) ^ g) * 16;
#pragma unroll
        for (int i = 0; i < 8; ++i) dma(X, HI0 + w * 8192, i * 1024, (xv + (i & 1) * 16384) ^ (i & 1) * 64, c * 32768 + ((i >> 1) & 1) * 4096 + (2 * w + (i >> 2)) * 128, 0);
    }
#pragma unroll
    for (int u = 0; u < SNR; ++u) S.issue(0, 0, u);
    cf l0, l1;
    {
        const int p = (w * 64 + l) * 2;
        const v4f t0 = lam_coef(a, p), t1 = lam_coef(a, p + 1);
        *LDS(v4f, LTAB + p * 16) = t0; *LDS(v4f, LTAB + p * 16 + 16) = t1;
        l0 = {t0[0], t0[1]}; l1 = {t1[0], t1[1]};
#pragma unroll
        for (int j = 0; j < 5; ++j) { l0 = cmul(l0, l0); l1 = cmul(l1, l1); }
    }
    vwait<4 * (SNR - 1)>(); bar();
    v4f br[8][2] = {}, bi[8][2] = {};
    v8bf f[2][4], x[2][4];
    auto xread = [&](int kk, v8bf (&d)[4]) {
        const u32 o = HI0 + ((kk + S.rot) & 7) * 4096;
#pragma unroll
        for (int i = 0; i < 4; ++i) d[i] = *LDS(v8bf, (fa ^ (i & 1) * 64) + o + (i >> 1) * 2048);
    };
    S.read(f[0]); xread(0, x[0]);
    for (int kk = 0; kk < 8; ++kk) {
#pragma unroll
        for (int t = 0; t < 8; ++t) {
            vwait<4 * (SNR - 2)>();
            S.read(f[(t + 1) & 1]);
            if (t == 7) { xread(kk + 1, x[1]); lwaitn<8>(); } else lwaitn<4>();
            if (t + SNR < 8) S.issue(0, kk, t + SNR); else if (kk < 7) S.issue(0, kk + 1, t + SNR - 8); else S.issue(1, (t + SNR - 8) >> 2, (t + SNR - 8) & 3);
#pragma unroll
            for (int u = 0; u < 2; ++u)
#pragma unroll
                for (int m = 0; m < 2; ++m) { br[t][m] = mm(x[0][2 * m + u], f[t & 1][u], br[t][m]); bi[t][m] = mm(x[0][2 * m + u], f[t & 1][2 + u], bi[t][m]); }
        }
#pragma unroll
        for (int i = 0; i < 4; ++i) x[0][i] = x[1][i];
    }
    cf lam[8], ex[8], pw[8];
#pragma unroll
    for (int t = 0; t < 8; ++t) {
        const v4f tb = *LDS(v4f, LTAB + (w * 128 + t * 16 + n) * 16);
        const cf co = {tb[2], tb[3]}; lam[t] = {tb[0], tb[1]};
        cf hh = {0.f, 0.f};
#pragma unroll
        for (int j = 0; j < 8; ++j) { hh = cfma(lam[t], hh, cmul(co, cf{br[t][j >> 2][j & 3], bi[t][j >> 2][j & 3]})); br[t][j >> 2][j & 3] = hh.x; bi[t][j >> 2][j & 3] = hh.y; }
        cf l8 = cmul(lam[t], lam[t]); l8 = cmul(l8, l8); l8 = cmul(l8, l8);
        const cf l16 = cmul(l8, l8);
        const cf odd = pick(g & 1, rows<0>(hh), cf{0.f, 0.f}), lg = pick(g & 1, l8, cf{1.f, 0.f});
        hh = cfma(l8, odd, hh);
        const cf p1 = half0(rows<1>(hh));
        hh = cfma(l16, p1, hh);
        ex[t] = cfma(lg, pick(g & 2, p1, cf{0.f, 0.f}), odd);
        pw[t] = pick(g & 2, cmul(lg, l16), lg);
        if (g == 3) __builtin_amdgcn_raw_buffer_store_b64(v2u{BC(u32, hh.x), BC(u32, hh.y)}, R, (w * 128 + t * 16 + n) * 8, EST + c * 4096, SC1);
    }
    vwait<0>(); bar();
    if (w == 0) {
        if (l == 0) st1(1, R, 0, SFLAG + c * 4);
        for (int it = 0; it < SPIN; ++it) {
            const int fl = (int)ld1(R, l * 4, SFLAG);
            if (!__builtin_amdgcn_ballot_w64(l < c && fl != 1)) break;
            __builtin_amdgcn_s_sleep(16);
        }
    }
    bar();
    {
        const int p = (w * 64 + l) * 2;
        cf h0 = {0.f, 0.f}, h1 = {0.f, 0.f};
        v4u e[16];
#pragma unroll
        for (int j = 0; j < 16; ++j) { e[j] = ld4(R, p * 8, EST + j * 4096); __builtin_amdgcn_sched_barrier(0); }
        for (int j0 = 0; j0 < c; j0 += 16)
#pragma unroll
            for (int j = 0; j < 16; ++j) {
                if (j0 + j < c) { h0 = cfma(l0, h0, cf{__uint_as_float(e[j][0]), __uint_as_float(e[j][1])}); h1 = cfma(l1, h1, cf{__uint_as_float(e[j][2]), __uint_as_float(e[j][3])}); }
                e[j] = ld4(R, p * 8, EST + min(j0 + j + 16, 63) * 4096); __builtin_amdgcn_sched_barrier(0);
            }
        *LDS(v4f, HIN + p * 8) = v4f{h0.x, h0.y, h1.x, h1.y};
    }
#pragma unroll
    for (int t = 0; t < 8; ++t) {
        const int p = w * 128 + t * 16 + n;
        const v2f hin = *LDS(v2f, HIN + p * 8);
        cf cy = cfma(pw[t], cf{hin[0], hin[1]}, ex[t]);
#pragma unroll
        for (int j = 0; j < 8; ++j) { cy = cmul(lam[t], cy); br[t][j >> 2][j & 3] += cy.x; bi[t][j >> 2][j & 3] += cy.y; }
        *LDS(v8bf, HI0 + p * 64 + g * 16) = pk8(br[t][0], br[t][1]); *LDS(v8bf, HI0 + 32768 + p * 64 + g * 16) = pk8(-bi[t][0], -bi[t][1]);
    }
    v4bf dk[8], xv[8][2];
#pragma unroll
    for (int t = 0; t < 8; ++t) {
        const int d = w * 128 + t * 16 + g * 4;
        dk[t] = *(const v4bf*)(a.dsk + d);
#pragma unroll
        for (int m = 0; m < 2; ++m) xv[t][m] = *(const v4bf*)(a.ax + (c * 32 + m * 16 + n) * 512 + d);
    }
    bar();
    v4f y[8][2] = {};
    auto hread = [&](int kk, v8bf (&d)[4]) {
        const u32 o = HI0 + (((kk + S.rot) & 15) * 64 + g * 8 + ((l >> 2) & 3)) * 64 + (l & 3) * 8;
#pragma unroll
        for (int i = 0; i < 4; ++i) d[i] = cat(trd(o + (i & 1) * 2048 + (i >> 1) * 32), trd(o + (i & 1) * 2048 + (i >> 1) * 32 + 256));
    };
    hread(0, x[0]);
    auto pass = [&](int kk, int last) {
#pragma unroll
        for (int t = 0; t < 4; ++t) {
            if (last != 2) vwait<4 * (SNR - 2)>(); else if (t == 0) vwait<8>(); else if (t == 1) vwait<4>(); else vwait<0>();
            if (last != 2 || t < 3) S.read(f[(t + 1) & 1]);
            if (t == 3 && last != 2) { hread(kk + 1, x[1]); lwaitn<12>(); } else lwaitn<4>();
            if (last == 0 || (last == 1 && t < 3)) S.issue(1, kk + ((t + SNR) >> 2), (t + SNR) & 3);
#pragma unroll
            for (int u = 0; u < 2; ++u)
#pragma unroll
                for (int m = 0; m < 2; ++m) { y[2 * t][m] = mm(f[t & 1][u], x[0][2 * m + u], y[2 * t][m]); y[2 * t + 1][m] = mm(f[t & 1][2 + u], x[0][2 * m + u], y[2 * t + 1][m]); }
        }
#pragma unroll
        for (int i = 0; i < 4; ++i) x[0][i] = x[1][i];
    };
    for (int kk = 0; kk < 14; ++kk) pass(kk, 0);
    pass(14, 1); pass(15, 2);
#pragma unroll
    for (int t = 0; t < 8; ++t)
#pragma unroll
        for (int m = 0; m < 2; ++m) {
            v4f o;
#pragma unroll
            for (int i = 0; i < 4; ++i) o[i] = 2.f * y[t][m][i] + rb((float)xv[t][m][i] * (float)dk[t][i]);
            __builtin_amdgcn_raw_buffer_store_b128(BC(v4u, o), rs(a.out, 8u << 20), ((c * 32 + m * 16 + n) * 1024 + w * 128 + t * 16 + g * 4) * 4, 0, SC1);
        }
}

constexpr int NPROBE = 8;
DEV u32 probe(rsrc_t R, u32 o, int l) {
    if (l < NPROBE) st1(0xffffffffu, R, l * 640, o);
    vwait<0>();
    u32 best = 0xffff;
#pragma unroll
    for (int i = 0; i < NPROBE; ++i) {
        unsigned long long t0, t1; u32 x;
        asm volatile("s_memtime %0\n buffer_load_dword %2, off, %3, %4 sc1\n s_waitcnt vmcnt(0)\n s_memtime %1\n s_waitcnt lgkmcnt(0)"
                     : "=&s"(t0), "=&s"(t1), "=&v"(x) : "s"(R), "s"(o + i * 640) : "memory");
        best = min(best, (u32)(t1 - t0));
    }
    return best;
}

DEV void pause(u32 ticks) {
    const u32 t0 = (u32)__builtin_amdgcn_s_memrealtime();
    while ((u32)__builtin_amdgcn_s_memrealtime() - t0 < ticks) __builtin_amdgcn_s_sleep(8);
}
DEV int lane() { int x; asm volatile("v_mbcnt_lo_u32_b32 %0, -1, 0\n v_mbcnt_hi_u32_b32 %0, -1, %0" : "=v"(x)); return x; }
typedef const __attribute__((address_space(4))) Args* argp_t;
DEV Args args() { argp_t p = (argp_t)__builtin_amdgcn_kernarg_segment_ptr(); asm volatile("" : "+s"(p));
#if __HIP_DEVICE_COMPILE__
    return *p;
#else
    return Args{};
#endif
}

DEV void warm(u32 xcc, int k, int w, int l, int i0, int i1) {
    const Args a = args();
    for (int t = i0 * 3 + w - 1; t < min(i1 * 3, xcc < 4 ? 96 : 64); t += 3) {
        const int m = t >> 5, g = (t & 31) * 8 + k - 24;
        const u32 v = m == 2 ? l * 64 : (l >> 5) * 8192 + (l & 31) * 64, s = m == 2 ? (xcc & 3) * 1048576 + g * 4096 : (xcc & 3) * 2048 + g * 16384;
        asm volatile("s_mov_b32 m0, 4096\n s_nop 0\n buffer_load_dword %0, %1, %2 offen lds" ::"v"(v), "s"(rs(m == 2 ? a.wo : m ? a.wq : a.wv, 4 << 20)), "s"(s) : "memory", "m0");
    }
}

DEV u32 startup(rsrc_t R, int b, u32 xcc, int w, int l) {
    const int j = b - 128, k = b >> 3;
    u32 m = 0, nonce = 0;
    if (w == 0) {
        m = (ld1(R, 0, GO + b * 128) + 256) >> 8;
        if (j == 0) {
            const u32 d0 = ld1(R, l * 128, DONE) >> 16, d1 = ld1(R, l * 128, DONE + 8192) >> 16;
            nonce = __builtin_amdgcn_readfirstlane(ld1(R, 0, NONCE));
            do nonce = (nonce + 1) & 0xffff; while (__builtin_amdgcn_ballot_w64(d0 == nonce || d1 == nonce));
            if (l == 0) st1(nonce, R, 0, NONCE);
        }
        if (l == 0) st1(m, R, 0, REQ + b * 128);
    }
    const v4u ones = {~0u, ~0u, ~0u, ~0u};
    const u32 v = (w * 64 + l) * 16;
    if (j < 64) {
        if (j)
#pragma unroll
            for (int i = 0; i < 8; ++i) st4(ones, R, v, SLOT0 + j * 32768 + i * 4096);
#pragma unroll
        for (int i = 0; i < 4; ++i) st4(ones, R, v, INB0 + (i >> 1 << 20) + j * 8192 + (i & 1) * 4096);
    }
    if (j < 9) st4(ones, R, v, FLAG0 + j * 4096);
    if (w && j >= 64) { warm(xcc, k, w, l, 0, 2); bar(); warm(xcc, k, w, l, 2, 32); }
    else { vwait<0>(); bar(); }
    if (w == 0) {
        u32 lat = 0, cfg = 0;
        if (xcc == 0 && k < 24) lat = min(probe(R, (k & 4) ? PROBE + ((k & 3) ^ 2) * 8192 : SLOT0 + (k & 3) * 8192, l), 2047u);
        const u32 rep = lat << 5 | xcc << 2 | (k & 3);
        if (j) {
            u32 last = ~0u;
            for (int it = 0; it < SPIN; ++it) {
                const u32 n = ld1(R, 0, NONCE) & 0xffff;
                cfg = ld1(R, 0, GO + b * 128);
                if (cfg >> 8 == m) break;
                if (n != last && l == 0) st1(n << 16 | rep, R, 0, DONE + j * 128);
                last = n;
                __builtin_amdgcn_s_sleep(4);
            }
        } else {
            u32 d0 = 0;
            for (int it = 0; it < SPIN; ++it) {
                d0 = ld1(R, l * 128, DONE);
                if (l == 0) d0 = nonce << 16 | rep;
                if (!__builtin_amdgcn_ballot_w64(d0 >> 16 != nonce)) break;
                __builtin_amdgcn_s_sleep(2);
            }
            const int j0 = (8 - xcc) & 7;
#define CW(i) (u32)__builtin_amdgcn_readlane(d0, j0 + 8 * (i))
            cfg = (min(min(CW(0), CW(1)), min(CW(2), CW(3))) & 3) | (min(min(CW(4), CW(5)), min(CW(6), CW(7))) & 3) << 2 | xcc << 4;
            v4u seen[4] = {};
            bool late = true;
            for (int it = 0; it < SPIN; ++it) {
                v4u q[4];
#pragma unroll
                for (int i = 0; i < 4; ++i) q[i] = ld4(R, l * 128, REQ + i * 8192);
                const v4u a0 = ld4(R, l * 128, ACK), a1 = ld4(R, l * 128, ACK + 8192), o = a0 | a1;
                const u32 d1 = ld1(R, l * 128, DONE + 8192);
#pragma unroll
                for (int i = 0; i < 4; ++i) {
                    const v4u d = q[i] ^ seen[i];
                    if (it == 0 || (d[0] | d[1] | d[2] | d[3])) st4(q[i] << 8 | cfg, R, l * 128, GO + i * 8192);
                    seen[i] = q[i];
                }
                if (!late && !__builtin_amdgcn_ballot_w64(o[0] | o[1] | o[2] | o[3])) break;
                late = __builtin_amdgcn_ballot_w64(d1 >> 16 != nonce);
                __builtin_amdgcn_s_sleep(4);
            }
        }
        *LDS(u32, 0) = cfg;
    }
    vwait<0>(); bar();
    const u32 cfg = __builtin_amdgcn_readfirstlane(*LDS(u32, 0)) & 0x7f;
    bar();
    return cfg;
}

__global__ void __launch_bounds__(256, 1) fused(Args a0) {
    char* ws = a0.ws;
    const int b = blockIdx.x, k = b >> 3, w = __builtin_amdgcn_readfirstlane(threadIdx.x >> 6);
    const u32 xcc = __builtin_amdgcn_s_getreg(20 | (3 << 11)) & 7;
    if (k < 16) {
        u32 cfg = ((xcc - b) & 7) << 4;
        do {
            const int x = (b + (cfg >> 4)) & 7;
            const u32 id = w | ((x & 3) * 8 + (k >> 1)) << 2 | (k & 1) << 7 | cfg << 8 | b << 16;
            if (x < 4) {
                asm volatile(CHAIN_ASM : [redo] "=s"(cfg)
                    : [id] "s"(id), [da] "n"(T_DA), [db] "n"(T_P), [tq] "n"(T_Q), [tqm] "n"(0xffffffffu / (u32)(T_Q)), [ka] "s"(__builtin_amdgcn_kernarg_segment_ptr())
                    : CHAIN_CLOBBERS);
            } else {
                asm volatile(XROLE_ASM : [redo] "=s"(cfg)
                    : [id] "s"(id), [ka] "s"(__builtin_amdgcn_kernarg_segment_ptr())
                    : CHAIN_CLOBBERS);
            }
        } while (cfg);
    } else {
        const u32 cfg = startup(rs(ws, WS_BYTES), b, xcc, w, lane());
        const int x = (b + (cfg >> 4)) & 7;
        if (x >= 4) {
            { pause(350); srole(args(), ws, (x - 4) * 16 + (k - 16), w, lane()); bar(); }
            orole(args(), ws, x - 4, k - 16, cfg, w, lane());
        }
    }
}

extern "C" void kernel_launch(void* const* d_in, const int* in_sizes, int n_in, void* d_out, int out_size, void* d_ws, size_t ws_size, hipStream_t stream) {
    const u16* const* in = (const u16* const*)d_in;
    char* ws = (char*)(((size_t)d_ws + (2u << 20) - 1) & ~(size_t)((2u << 20) - 1));
    Args a{in[0], in[1], in[2], in[3], in[4], in[5], in[6], in[7], in[8], in[9], in[10], in[11], in[12], in[13], in[14], (float*)d_out, ws};
    (void)hipFuncSetAttribute((const void*)fused, hipFuncAttributeMaxDynamicSharedMemorySize, 163840);
    fused<<<256, 256, 163840, stream>>>(a);
}
```
